```python
import torch

def _softmax(x, axis=-1):
    return torch.softmax(x, dim=axis)
def _gelu(x):
    return torch.nn.functional.gelu(x, approximate="tanh")
def _silu(x):
    return torch.nn.functional.silu(x)
def _sigmoid(x):
    return torch.sigmoid(x)
def _layernorm(x, axis=-1, eps=1e-5):
    m = x.mean(dim=axis, keepdim=True)
    v = x.var(dim=axis, keepdim=True, unbiased=False)
    return (x - m) / torch.sqrt(v + eps)
def _scatter_add(tgt, idx, src, axis=0):
    return tgt.clone().index_add_(axis, idx, src)
def _segment_sum(src, seg, n):
    out = torch.zeros((n,) + src.shape[1:], dtype=src.dtype)
    return out.index_add_(0, seg, src)
def _pad(x, pw):
    flat = tuple(v for pair in reversed(pw) for v in pair)
    return torch.nn.functional.pad(x, flat)

kernel_name = 'sequential_preLN__ssm_conv_s4__geglu_ffn__prompt'

def setup_inputs():

    _w = (lambda a: (lambda t: t.to(torch.bfloat16) if t.is_floating_point() else t)(torch.as_tensor(a)))
    def _A_setup():
        B, L, D, N = 1, 2048, 448, 256
        A_re = -(lambda _x: torch.exp(torch.as_tensor(_x)))((lambda *s: torch.randn(*s, dtype=torch.bfloat16) * 3.2)(D, N))
        A_im = torch.pi * (lambda *s: torch.randn(*s, dtype=torch.bfloat16) * 0.05)(D, N)
        B_re = (lambda *s: torch.randn(*s, dtype=torch.bfloat16) * 0.05)(D, N)
        B_im = (lambda *s: torch.randn(*s, dtype=torch.bfloat16) * 0.05)(D, N)
        C_re = (lambda *s: torch.randn(*s, dtype=torch.bfloat16) * 0.05)(D, N)
        C_im = (lambda *s: torch.randn(*s, dtype=torch.bfloat16) * 0.05)(D, N)
        log_dt = (lambda *s: torch.randn(*s, dtype=torch.bfloat16) * 0.05)(D) * 0.5 - 4.0
        Dskip = (lambda *s: torch.randn(*s, dtype=torch.bfloat16) * 0.05)(D)
        return {
            "u": _w((lambda *s: torch.randn(*s, dtype=torch.bfloat16) * 0.05)(B, L, D)),
            "A_re": _w(A_re),
            "A_im": _w(A_im),
            "B_re": _w(B_re),
            "B_im": _w(B_im),
            "C_re": _w(C_re),
            "C_im": _w(C_im),
            "log_dt": _w(log_dt),
            "Dskip": _w(Dskip),
        }
    _A_in = _A_setup()

    def _B_setup():
        B, S, D, F = 1, 2048, 448, 1792
        return {
            "x": _w((lambda *s: torch.randn(*s, dtype=torch.bfloat16) * 0.05)(B, S, D)),
            "w_gate": _w((lambda *s: torch.randn(*s, dtype=torch.bfloat16) * 0.05)(D, F)),
            "w_up": _w((lambda *s: torch.randn(*s, dtype=torch.bfloat16) * 0.05)(D, F)),
            "w_down": _w((lambda *s: torch.randn(*s, dtype=torch.bfloat16) * 0.05)(F, D)),
        }
    _B_in = _B_setup()

    return {
        "A_u": _A_in["u"],
        "A_A_re": _A_in["A_re"],
        "A_A_im": _A_in["A_im"],
        "A_B_re": _A_in["B_re"],
        "A_B_im": _A_in["B_im"],
        "A_C_re": _A_in["C_re"],
        "A_C_im": _A_in["C_im"],
        "A_log_dt": _A_in["log_dt"],
        "A_Dskip": _A_in["Dskip"],
        "B_x": _B_in["x"],
        "B_w_gate": _B_in["w_gate"],
        "B_w_up": _B_in["w_up"],
        "B_w_down": _B_in["w_down"],
    }

def reference(A_u, A_A_re, A_A_im, A_B_re, A_B_im, A_C_re, A_C_im, A_log_dt, A_Dskip, B_x, B_w_gate, B_w_up, B_w_down):
    def _A_ref(u, A_re, A_im, B_re, B_im, C_re, C_im, log_dt, Dskip):
        Bsz, L, D = u.shape
        dt = torch.exp(log_dt)[:, None]
        A = A_re + 1j * A_im
        Bc = B_re + 1j * B_im
        Cc = C_re + 1j * C_im
        logdA = dt * A
        dB = dt * Bc
        l = (lambda x, dt: x.to(dt))(torch.arange(L), A_re.dtype)
        P = torch.exp(logdA[:, :, None] * l[None, None, :])
        K = torch.einsum("dn,dnl,dn->dl", Cc, P, dB).real
        L2 = 2 * L
        Kp = _pad(K, ((0, 0), (0, L)))
        up = (lambda x, *d: x.permute(*d))(u, 0, 2, 1)
        up = _pad(up, ((0, 0), (0, 0), (0, L)))
        Kf = (lambda x, axes=(-2,-1): torch.fft.rfft2(x.float(), dim=axes))(Kp[:, None, :], axes=(-2, -1))
        Uf = (lambda x, axes=(-2,-1): torch.fft.rfft2(x.float(), dim=axes))(up[:, :, None, :], axes=(-2, -1))
        Yf = Uf * Kf[None, :, :, :]
        y = (lambda x, s, axes=(-2,-1): torch.fft.irfft2(x, s=s, dim=axes).to(torch.bfloat16))(Yf, (1, L2), axes=(-2, -1))[:, :, 0, :L]
        y = (lambda x, *d: x.permute(*d))(y, 0, 2, 1) + u * Dskip[None, None, :]
        return y

    def _B_ref(x, w_gate, w_up, w_down):
        return (_gelu(x @ w_gate) * (x @ w_up)) @ w_down

    x = A_u
    h = x + _A_ref(_layernorm(x, axis=-1), A_A_re, A_A_im, A_B_re, A_B_im, A_C_re, A_C_im, A_log_dt, A_Dskip)
    h2 = h + _B_ref(_layernorm(h, axis=-1), B_w_gate, B_w_up, B_w_down)
    return h2
```

```cpp
#include <hip/hip_runtime.h>
#include <hip/hip_bf16.h>
#include <stdint.h>
#define ROUND_L 0
typedef unsigned short u16;
__device__ __forceinline__ float bf2f(u16 v){ return __uint_as_float(((unsigned)v)<<16); }
__device__ __forceinline__ u16 f2bf(float f){ unsigned u=__float_as_uint(f); u += 0x7fffu + ((u>>16)&1u); return (u16)(u>>16); }
__device__ __forceinline__ float rbf(float f){ return bf2f(f2bf(f)); }
constexpr int L=2048, D=448, N=256, F=1792;

__global__ void k_ln(const u16* x, u16* u){
  int t=blockIdx.x; int lane=threadIdx.x;
  float v[7]; float s=0;
  for(int i=0;i<7;i++){ v[i]=bf2f(x[t*D+lane+64*i]); s+=v[i]; }
  for(int o=32;o>0;o>>=1) s+=__shfl_xor(s,o);
  float mean=s/D; float q=0;
  for(int i=0;i<7;i++){ float d=v[i]-mean; q+=d*d; }
  for(int o=32;o>0;o>>=1) q+=__shfl_xor(q,o);
  float var=rbf(q/D); float m=rbf(mean);
  float sd=rbf(sqrtf(rbf(var+1e-5f)));
  for(int i=0;i<7;i++){ u[t*D+lane+64*i]=f2bf(rbf(v[i]-m)/sd); }
}
__global__ void k_kgen(const u16* Are,const u16* Aim,const u16* Bre,const u16* Bim,const u16* Cre,const u16* Cim,const u16* logdt,float* K){
  int d=blockIdx.y; int l=blockIdx.x*256+threadIdx.x;
  float dt=rbf(expf(bf2f(logdt[d])));
  float lf = ROUND_L ? rbf((float)l) : (float)l;
  float acc=0;
  for(int n=0;n<N;n++){
    float a=dt*bf2f(Are[d*N+n]), b=dt*bf2f(Aim[d*N+n]);
    float br=dt*bf2f(Bre[d*N+n]), bi=dt*bf2f(Bim[d*N+n]);
    float cr=bf2f(Cre[d*N+n]), ci=bf2f(Cim[d*N+n]);
    float wr=cr*br-ci*bi, wi=cr*bi+ci*br;
    float e=expf(a*lf); float s,c; sincosf(b*lf,&s,&c);
    acc += e*(wr*c-wi*s);
  }
  K[d*L+l]=acc;
}
__global__ void k_conv(const u16* x,const u16* u,const float* K,const u16* Dskip,u16* h){
  int t=blockIdx.x; int d=threadIdx.x;
  float acc=0;
  for(int l=0;l<=t;l++) acc += K[d*L+l]*bf2f(u[(t-l)*D+d]);
  float y=rbf(acc); float ud=rbf(bf2f(u[t*D+d])*bf2f(Dskip[d]));
  float y2=rbf(y+ud);
  h[t*D+d]=f2bf(bf2f(x[t*D+d])+y2);
}
__global__ void k_g1(const u16* u2,const u16* wg,const u16* wu,u16* act){
  int t=blockIdx.y; int f=blockIdx.x*256+threadIdx.x;
  float g=0,up=0;
  for(int k=0;k<D;k++){ float a=bf2f(u2[t*D+k]); g+=a*bf2f(wg[k*F+f]); up+=a*bf2f(wu[k*F+f]); }
  g=rbf(g); up=rbf(up);
  float ge=0.5f*g*(1.f+tanhf(0.7978845608028654f*(g+0.044715f*g*g*g)));
  act[t*F+f]=f2bf(rbf(ge)*up);
}
__global__ void k_g2(const u16* act,const u16* wd,const u16* h,u16* out){
  int t=blockIdx.x; int d=threadIdx.x;
  float o=0;
  for(int k=0;k<F;k++) o+=bf2f(act[t*F+k])*bf2f(wd[k*D+d]);
  out[t*D+d]=f2bf(bf2f(h[t*D+d])+rbf(o));
}
extern "C" void kernel_launch(void* const* d_in, const int* in_sizes, int n_in,
                              void* d_out, int out_size, void* d_ws, size_t ws_size,
                              hipStream_t stream) {
  const u16* x=(const u16*)d_in[0];
  char* ws=(char*)d_ws;
  u16* u=(u16*)ws; u16* h=(u16*)(ws+(4<<20)); u16* u2=(u16*)(ws+(8<<20)); float* K=(float*)(ws+(12<<20)); u16* act=(u16*)(ws+(20<<20));
  k_ln<<<L,64,0,stream>>>(x,u);
  k_kgen<<<dim3(L/256,D),256,0,stream>>>((const u16*)d_in[1],(const u16*)d_in[2],(const u16*)d_in[3],(const u16*)d_in[4],(const u16*)d_in[5],(const u16*)d_in[6],(const u16*)d_in[7],K);
  k_conv<<<L,D,0,stream>>>(x,u,K,(const u16*)d_in[8],h);
  k_ln<<<L,64,0,stream>>>(h,u2);
  k_g1<<<dim3(F/256,L),256,0,stream>>>(u2,(const u16*)d_in[10],(const u16*)d_in[11],act);
  k_g2<<<L,D,0,stream>>>(act,(const u16*)d_in[12],h,(u16*)d_out);
}
```

```cpp
#include <hip/hip_runtime.h>
#include <stdint.h>
typedef unsigned short u16; typedef unsigned u32; typedef unsigned long long u64;
typedef __attribute__((ext_vector_type(4))) unsigned u32x4;
typedef __attribute__((ext_vector_type(2))) unsigned u32x2;
typedef __attribute__((ext_vector_type(4))) short s16x4;
typedef __attribute__((ext_vector_type(2))) __bf16 bf16x2;
typedef __attribute__((ext_vector_type(8))) __bf16 bf16x8;
typedef __attribute__((ext_vector_type(2))) float f32x2;
typedef __attribute__((ext_vector_type(4))) float f32x4;
typedef __attribute__((ext_vector_type(16))) float f32x16;
#define DI __device__ __forceinline__
#define LDSP(T, off) ((__attribute__((address_space(3))) T*)(off))
#define GAS __attribute__((address_space(1)))
constexpr int L=2048, D=448, N=256, F=1792;

DI float bf2f(u16 v){ return __uint_as_float(((unsigned)v)<<16); }
DI u16 f2bf(float f){ unsigned u=__float_as_uint(f); u += 0x7fffu + ((u>>16)&1u); return (u16)(u>>16); }
DI float rbf(float f){ return bf2f(f2bf(f)); }
DI u32 pkbf(float a,float b){ f32x2 v={a,b}; return __builtin_bit_cast(u32,__builtin_convertvector(v,bf16x2)); }
DI float dot2bf(u32 a,u32 b,float c){ return __builtin_amdgcn_fdot2_f32_bf16(__builtin_bit_cast(bf16x2,a),__builtin_bit_cast(bf16x2,b),c,false); }
DI f32x16 mfma32(u32x4 a,u32x4 b,f32x16 c){ return __builtin_amdgcn_mfma_f32_32x32x16_bf16(__builtin_bit_cast(bf16x8,a),__builtin_bit_cast(bf16x8,b),c,0,0,0); }
DI u32x2 trread(u32 addr){ return __builtin_bit_cast(u32x2,__builtin_amdgcn_ds_read_tr16_b64_v4i16(LDSP(s16x4,addr))); }
DI void glds16(const void* gsrc, u32 lds_dst){
  u32 keep;
  asm volatile("s_mov_b32 %0, m0\n\ts_mov_b32 m0, %2\n\ts_nop 0\n\tglobal_load_lds_dwordx4 %1, off\n\ts_mov_b32 m0, %0" : "=&s"(keep) : "v"(gsrc), "s"(lds_dst) : "memory");
}
#define WAITV(n) asm volatile("s_waitcnt vmcnt(" #n ")" ::: "memory")
#define WAITL(n) asm volatile("s_waitcnt lgkmcnt(" #n ")" ::: "memory")
#define BAR() __builtin_amdgcn_s_barrier()

#define STAMP_DECL
#define STAMP_FLUSH(k)
#define STAMP(k,i)
constexpr size_t OFF_H   = 0;
constexpr size_t OFF_W1  = 2u<<20;
constexpr size_t OFF_WD  = 6u<<20;
constexpr size_t OFF_ACT = 8u<<20;
constexpr size_t OFF_GR  = 16u<<20;
constexpr size_t OFF_EP  = OFF_GR+65536;

DI int hswz(int d){ return ((d&3)<<2)|((d>>2)&3); }
DI void prep_body(int p, const u16* wg, const u16* wu, const u16* wd, u32x4* W1f, u32x4* Wdf){
  const int w=__builtin_amdgcn_readfirstlane(threadIdx.x>>6), lane=threadIdx.x&63;
  const int row=8*w+(lane>>3), chp=lane&7;
  const int fsw=(((row>>1)&1)<<1)|((row>>5)&1);
  const int ch=chp^(fsw<<1);
  #pragma unroll 1
  for(int it=0;it<19;it++){
    int u=p+32*it; if(u>=588) break;
    const u16* src;
    if(u<392){ int kc=u/28, fc=u%28; const u16* base=(row<32)?wg:wu; src=base+(size_t)(kc*32+(row&31))*F+fc*64+ch*8; }
    else { int v=u-392; int fc=v/7, dc=v%7; src=wd+(size_t)(fc*64+row)*D+dc*64+ch*8; }
    glds16(src, it*8192+w*1024);
  }
  WAITV(0); BAR();
  const int g16=lane>>4, h=g16>>1, sub=g16&1, q=(lane>>2)&3, pp=lane&3;
  #pragma unroll 1
  for(int it=0;it<19;it++){
    int u=p+32*it; if(u>=588) break;
    u32x2 r[2];
    u32x4* dst;
    if(u<392){ int kc=u/28, fc=u%28; int wsub=w&3, sp=w>>2; int wq=(fc&1)*4+wsub;
      #pragma unroll
      for(int e=0;e<2;e++){ int rr=sub*32+sp*16+8*h+4*(e^(wq>>2))+q; int c=wsub*2+(pp>>1); int f2=(((rr>>1)&1)<<1)|((rr>>5)&1);
        r[e]=trread(it*8192+rr*128+((c^(f2<<1))<<4)+8*(pp&1)); }
      dst=W1f+((size_t)(((fc>>1)*8+wq)*28+2*kc+sp))*64+lane;
    } else { int v=u-392; int fc=v/7, dc=v%7; int sg=w&3, dl=w>>2;
      #pragma unroll
      for(int e=0;e<2;e++){ int rr=sg*16+8*e+4*h+q; int c=dl*4+sub*2+(pp>>1); int f2=(((rr>>1)&1)<<1)|((rr>>5)&1);
        r[e]=trread(it*8192+rr*128+((c^(f2<<1))<<4)+8*(pp&1)); }
      dst=Wdf+((size_t)((2*dc+dl)*112+4*fc+sg))*64+lane;
    }
    u32x4 o={r[0][0],r[0][1],r[1][0],r[1][1]};
    *dst=o;
  }
  WAITV(0);
}
typedef __attribute__((ext_vector_type(2))) _Float16 f16x2;
typedef __attribute__((ext_vector_type(8))) _Float16 f16x8;
DI u32 pkh(float a,float b){ f32x2 v={a,b}; return __builtin_bit_cast(u32,__builtin_convertvector(v,f16x2)); }
DI f32x16 mfma32h(u32x4 a,u32x4 b,f32x16 c){ return __builtin_amdgcn_mfma_f32_32x32x16_f16(__builtin_bit_cast(f16x8,a),__builtin_bit_cast(f16x8,b),c,0,0,0); }
DI f32x4 mfma16h(u32x4 a,u32x4 b,f32x4 c){ return __builtin_amdgcn_mfma_f32_16x16x32_f16(__builtin_bit_cast(f16x8,a),__builtin_bit_cast(f16x8,b),c,0,0,0); }
template<int CTRL,int RM> DI float dppf(float v){ return __builtin_bit_cast(float,__builtin_amdgcn_update_dpp(0,__builtin_bit_cast(int,v),CTRL,RM,0xf,false)); }
DI float wave_sum(float v){
  v+=dppf<0x111,0xf>(v); v+=dppf<0x112,0xf>(v); v+=dppf<0x114,0xf>(v); v+=dppf<0x118,0xf>(v);
  v+=dppf<0x142,0xa>(v); v+=dppf<0x143,0xc>(v);
  return __builtin_bit_cast(float,__builtin_amdgcn_readlane(__builtin_bit_cast(int,v),63));
}
DI float wave_max_pos(float v){
  v=fmaxf(v,dppf<0x111,0xf>(v)); v=fmaxf(v,dppf<0x112,0xf>(v)); v=fmaxf(v,dppf<0x114,0xf>(v)); v=fmaxf(v,dppf<0x118,0xf>(v));
  v=fmaxf(v,dppf<0x142,0xa>(v)); v=fmaxf(v,dppf<0x143,0xc>(v));
  return __builtin_bit_cast(float,__builtin_amdgcn_readlane(__builtin_bit_cast(int,v),63));
}
struct cf{ float re,im; };
DI cf cmul(cf a,cf b){ cf r; r.re=__builtin_fmaf(a.re,b.re,-(a.im*b.im)); r.im=__builtin_fmaf(a.re,b.im,a.im*b.re); return r; }
DI cf cexpz(float a2,float br,float l){ float e=__builtin_amdgcn_exp2f(a2*l); float p=__builtin_amdgcn_fractf(br*l); cf r; r.re=e*__builtin_amdgcn_cosf(p); r.im=e*__builtin_amdgcn_sinf(p); return r; }

constexpr int TS=272;
constexpr int TW=53*TS;
constexpr int S_U   = 8*TW;
constexpr int UPAD=576, UB=42*144;
constexpr int S_KR  = S_U+2*UB;
constexpr int KRB=1216;
constexpr int S_CT  = S_KR+4*KRB;
constexpr int CTB=192;
constexpr int S_EX  = S_CT+2*6*2*CTB;
constexpr int S_END = S_EX+64;
constexpr int YDB=32*272;
constexpr int S_YD=0, S_YC=4*YDB;
constexpr int S_LDS = 19*8192;
static_assert(S_END<=S_LDS,"lds");
static_assert(S_YC+4*6144<=S_U,"lds");
DI int uaddr(int s){ s+=UPAD; return 2*s+16*(s>>6); }

struct SArgs{ const u16 *x,*Are,*Aim,*Bre,*Bim,*Cre,*Cim,*logdt,*Dskip,*wg,*wu,*wd; char* ws; };

__global__ __launch_bounds__(512,2) void k_s(SArgs a){
  __shared__ __attribute__((aligned(16))) char smem[S_LDS];
  asm volatile("" :: "v"(smem));
  STAMP_DECL; STAMP(0,0);
  const int b=blockIdx.x, tid=threadIdx.x;
  const int w=__builtin_amdgcn_readfirstlane(tid>>6), lane=tid&63;
  auto rsX=__builtin_amdgcn_make_buffer_rsrc((void*)a.x,0,L*D*2,0x00020000);
  auto rsG=__builtin_amdgcn_make_buffer_rsrc((void*)(a.ws+OFF_GR),0,L*8,0x00020000);
  const int row=b*8+w;
  u32x4 xr=__builtin_amdgcn_raw_buffer_load_b128(rsX,lane<56?lane*16:0x7ffffff0,row*(D*2),0);
  u32 epoch=__hip_atomic_load((GAS u32*)(a.ws+OFF_EP),__ATOMIC_RELAXED,__HIP_MEMORY_SCOPE_AGENT);
  const bool isS=b<224;
  const int d0=isS?(56*(b&7)+2*(b>>3)):0;
  const int ch=w>>2, n=tid&255, d=d0+ch;
  u16 pAr,pAi,pBr,pBi,pCr,pCi,pdt; u32 pD; u32 xg[4];
  if(isS){
    const int o=d*N+n;
    pAr=a.Are[o]; pAi=a.Aim[o]; pBr=a.Bre[o]; pBi=a.Bim[o]; pCr=a.Cre[o]; pCi=a.Cim[o]; pdt=a.logdt[d];
    pD=*(const u32*)(a.Dskip+d0);
    #pragma unroll
    for(int i=0;i<4;i++) xg[i]=__builtin_amdgcn_raw_buffer_load_b32(rsX,tid*(4*D*2)+i*(D*2),d0*2,0);
    { u32x4 z={0,0,0,0};
      for(int o2=S_U+tid*16;o2<S_EX;o2+=512*16) *LDSP(u32x4,o2)=z; }
  }
  u32 tag;
  { float v[8];
    #pragma unroll
    for(int i=0;i<4;i++){ v[2*i]=__uint_as_float(xr[i]<<16); v[2*i+1]=__uint_as_float(xr[i]&0xffff0000u); }
    float s=((v[0]+v[1])+(v[2]+v[3]))+((v[4]+v[5])+(v[6]+v[7]));
    float mean=wave_sum(s)*(1.f/D);
    float q=0.f;
    #pragma unroll
    for(int i=0;i<8;i++){ float c=v[i]-mean; q=__builtin_fmaf(c,c,q); }
    q=lane<56?q:0.f;
    float var=wave_sum(q)*(1.f/D);
    float sd=__builtin_amdgcn_sqrtf(rbf(rbf(var)+1e-5f));
    tag=__builtin_amdgcn_readfirstlane(epoch)+1u;
    if(lane==0){ u64 g=((u64)tag<<32)|pkbf(mean,sd); __hip_atomic_store((GAS u64*)(a.ws+OFF_GR)+row,g,__ATOMIC_RELAXED,__HIP_MEMORY_SCOPE_AGENT); }
  }
  STAMP(0,1);
  if(!isS){
    prep_body(b-224,a.wg,a.wu,a.wd,(u32x4*)(a.ws+OFF_W1),(u32x4*)(a.ws+OFF_WD));
    STAMP(0,7); STAMP_FLUSH(0);
    return;
  }
  const int tb_=w*TW;
  { float dt=rbf(expf(bf2f(pdt)));
    float a2=(dt*bf2f(pAr))*1.4426950408889634f, br=(dt*bf2f(pAi))*0.15915494309189535f;
    cf B={dt*bf2f(pBr),dt*bf2f(pBi)}, C={bf2f(pCr),bf2f(pCi)};
    cf cb=cmul(C,B);
    float g=fmaxf(a2,0.f);
    float eg=__builtin_amdgcn_exp2f(248.f*g), e0=__builtin_amdgcn_exp2f(-248.f*g);
    float mag=(fabsf(cb.re)+fabsf(cb.im))*eg*__builtin_amdgcn_exp2f(2048.f*g);
    u32 ex=__float_as_uint(wave_max_pos(mag))>>23; ex=ex<6u?6u:(ex>254u?254u:ex);
    float sig=__uint_as_float((259u-ex)<<23), isig=__uint_as_float((ex-5u)<<23);
    if(lane==0) *LDSP(u32,S_EX+w*4)=ex;
    float sc=eg*sig; cb.re*=sc; cb.im*=sc;
    cf v1=cexpz(a2,br,1.f), w8=cexpz(a2,br,8.f), G=cexpz(a2,br,256.f);
    const int ta=tb_+lane*4;
    #define PUTX(r,val) (*LDSP(u32,ta+(r)*TS)=pkh((val).re,(val).im))
    #define PUTE(j,val) (*LDSP(u32,ta+(21+(j))*TS)=pkh((val).re,-(val).im))
    { cf w16=cmul(w8,w8); cf ea={e0,0.f}; cf eb={e0*w8.re,e0*w8.im};
      #pragma unroll
      for(int j=0;j<16;j++){ PUTE(2*j,ea); PUTE(2*j+1,eb); if(j<15){ ea=cmul(ea,w16); eb=cmul(eb,w16);} } }
    { cf v2=cmul(v1,v1), v4=cmul(v2,v2);
      cf x0=cb, x1=cmul(cb,v1), x2=cmul(x0,v2), x3=cmul(x1,v2), x4=cmul(x0,v4), x5=cmul(x1,v4), x6=cmul(x2,v4), x7=cmul(x3,v4);
      PUTX(0,x0);PUTX(1,x1);PUTX(2,x2);PUTX(3,x3);PUTX(4,x4);PUTX(5,x5);PUTX(6,x6);PUTX(7,x7);
      cf y=cmul(x0,G), y4=cmul(x4,G); PUTX(8,y); PUTX(9,cmul(x2,G)); PUTX(10,y4); PUTX(11,cmul(x6,G));
      y=cmul(y,G); y4=cmul(y4,G); PUTX(12,y); PUTX(13,y4);
      y=cmul(y,G); y4=cmul(y4,G); PUTX(14,y); PUTX(15,y4);
      #pragma unroll
      for(int m=0;m<5;m++){ y=cmul(y,G); PUTX(16+m,y); } }
    f32x16 acc; for(int r=0;r<16;r++) acc[r]=0.f;
    const int fa=tb_+(lane&31)*TS+(lane>>5)*16;
    #pragma unroll
    for(int s=0;s<8;s++){ u32x4 A=*LDSP(u32x4,fa+32*s), Bf=*LDSP(u32x4,fa+21*TS+32*s); acc=mfma32h(A,Bf,acc); }
    #pragma unroll
    for(int r=0;r<12;r++){ int rr=(r&3)+8*(r>>2); *LDSP(float,tb_+((rr+4*(lane>>5))*32+(lane&31))*4)=acc[r]*isig; }
  }
  STAMP(0,2);
  __syncthreads();
  float invS[2];
  { u32x4 e0=*LDSP(u32x4,S_EX), e1=*LDSP(u32x4,S_EX+16);
    u32 m0=max(max(e0[0],e0[1]),max(e0[2],e0[3])), m1=max(max(e1[0],e1[1]),max(e1[2],e1[3]));
    invS[0]=__uint_as_float((m0-5u)<<23); invS[1]=__uint_as_float((m1-5u)<<23);
    float SK=__uint_as_float((259u-(ch?m1:m0))<<23);
    const int pb=ch*4*TW, kr=S_KR+ch*2*KRB, ct=S_CT+ch*12*CTB;
    auto putk=[&](int l,_Float16 v){ int m=544-l; *LDSP(_Float16,kr+2*m)=v; *LDSP(_Float16,kr+KRB+2*m+2)=v; };
    auto putc=[&](int ty,int q,_Float16 v){ int m=64-q; *LDSP(_Float16,ct+ty*2*CTB+2*m)=v; *LDSP(_Float16,ct+(ty*2+1)*CTB+2*m+2)=v; };
    #pragma unroll
    for(int it=0;it<3;it++){
      int e=n+256*it; int r=e>>5, j=e&31;
      if(it<2||e<641){
        float val=(*LDSP(float,pb+e*4)+*LDSP(float,pb+TW+e*4))+(*LDSP(float,pb+2*TW+e*4)+*LDSP(float,pb+3*TW+e*4));
        _Float16 hv=(_Float16)(val*SK);
        if(it==0){ putk(r+8*j,hv); }
        else if(it==1){
          if(r<12){ int v=256+2*(r-8)+8*j; putk(v,hv); if(!(r&1)){ putk(v+1,hv); if(v>256) putk(v-1,hv);} }
          else { int i2=64*((r-12)>>1)+((r-12)&1)+2*j;
            if(i2==0){ putk(511,hv); *LDSP(_Float16,S_EX+32+ch*4)=hv; }
            else { int ph=(i2-1)&3, q2=(i2-1)>>2; putc((ph>>1)*3+(ph&1),q2,hv); } }
        } else {
          int i3=32*(r-16)+j;
          if(i3==0) *LDSP(_Float16,S_EX+32+ch*4+2)=hv;
          else { int ps=(i3-1)&1, q3=(i3-1)>>1; putc(ps*3+2,q3,hv); }
        }
      }
    }
  }
  __syncthreads();
  STAMP(0,3);
  const bool isD=((w&2)==0)==(ch==0);
  const int sub=w&1;
  u32x4 opA[17];
  if(isD){
    const int ka=S_KR+ch*2*KRB+(lane&1)*(KRB+2)+2*(32-(lane&31)+8*(lane>>5)+16*sub);
    #pragma unroll
    for(int i=0;i<17;i++){
      #pragma unroll
      for(int e=0;e<4;e++) opA[i][e]=*LDSP(u32,ka+64*i+4*e);
    }
  } else {
    const int rho=lane&15, th=lane>>4;
    const int ca=S_CT+ch*12*CTB+sub*6*CTB+(rho&1)*(CTB+2)+2*(64-rho+4*th);
    #pragma unroll
    for(int dl=0;dl<3;dl++){
      opA[dl][0]=*LDSP(u32,ca-32*dl); opA[dl][1]=*LDSP(u32,ca-32*dl+4);
      opA[dl][2]=*LDSP(u32,ca+2*CTB-32*dl); opA[dl][3]=*LDSP(u32,ca+2*CTB-32*dl+4);
    }
    #pragma unroll
    for(int dl=0;dl<4;dl++){
      opA[3+dl][0]=*LDSP(u32,ca+4*CTB-32*dl); opA[3+dl][1]=*LDSP(u32,ca+4*CTB-32*dl+4);
      if(dl==0){ opA[3][2]=0; opA[3][3]=0; } else { opA[3+dl][2]=opA[2+dl][0]; opA[3+dl][3]=opA[2+dl][1]; }
    }
    u32 sp=*LDSP(u16,S_EX+32+ch*4+2*sub);
    const int o=sub?-24:-16;
    #pragma unroll
    for(int ty=0;ty<4;ty++){
      int off,lo,hi;
      if(ty==0){ off=o+(sub?12:4); lo=-1; hi=1; }
      else if(ty==1){ off=o+(sub?16:8); lo=-2; hi=2; }
      else if(ty==2){ off=o+(sub?16:8); lo=sub?-4:-3; hi=sub?4:3; }
      else { off=sub?-8:-16; lo=sub?-4:-2; hi=sub?2:0; }
      u32 one=(ty==3)?sp:0x3c00u;
      #pragma unroll
      for(int e2=0;e2<4;e2++){
        int p0=8*th+2*e2+off-rho;
        u32 v0=(u32)(p0-lo)<=(u32)(hi-lo)?one:0u, v1=(u32)(p0+1-lo)<=(u32)(hi-lo)?one:0u;
        opA[7+ty][e2]=v0|(v1<<16);
      }
    }
    #pragma unroll
    for(int i=11;i<17;i++) opA[i]=opA[0];
  }
  float xf[2][4], ud[2][4];
  { u32x4 g0,g1; int spins=0;
    while(true){
      g0=__builtin_amdgcn_raw_buffer_load_b128(rsG,tid*32,0,16); g1=__builtin_amdgcn_raw_buffer_load_b128(rsG,tid*32+16,0,16);
      bool ok=(g0[1]==tag)&&(g0[3]==tag)&&(g1[1]==tag)&&(g1[3]==tag);
      if(__all(ok)||++spins>(1<<16)) break;
      __builtin_amdgcn_s_sleep(1);
      asm volatile("" ::: "memory");
    }
    STAMP(0,4);
    u32 st[4]={g0[0],g0[2],g1[0],g1[2]};
    float uu[2][4];
    const float D0=__uint_as_float(pD<<16), D1=__uint_as_float(pD&0xffff0000u);
    #pragma unroll
    for(int i=0;i<4;i++){
      float m=__uint_as_float(st[i]<<16), ri=__builtin_amdgcn_rcpf(__uint_as_float(st[i]&0xffff0000u));
      xf[0][i]=__uint_as_float(xg[i]<<16); xf[1][i]=__uint_as_float(xg[i]&0xffff0000u);
      u32 c=pkbf(xf[0][i]-m,xf[1][i]-m);
      u32 un=pkbf(__uint_as_float(c<<16)*ri,__uint_as_float(c&0xffff0000u)*ri);
      uu[0][i]=__uint_as_float(un<<16); uu[1][i]=__uint_as_float(un&0xffff0000u);
      u32 q=pkbf(uu[0][i]*D0,uu[1][i]*D1);
      ud[0][i]=__uint_as_float(q<<16); ud[1][i]=__uint_as_float(q&0xffff0000u);
    }
    const int ua=S_U+uaddr(4*tid);
    #pragma unroll
    for(int c=0;c<2;c++){ u32x2 v={pkh(uu[c][0],uu[c][1]),pkh(uu[c][2],uu[c][3])}; *LDSP(u32x2,ua+c*UB)=v; }
  }
  __syncthreads();
  if(b==0&&tid==0) __hip_atomic_store((GAS u32*)(a.ws+OFF_EP),tag,__ATOMIC_RELAXED,__HIP_MEMORY_SCOPE_AGENT);
  STAMP(0,5);
  if(isD){
    const int ub=S_U+ch*UB+144*(lane&31)+16*(lane>>5)+32*sub;
    f32x16 a0,a1; for(int r=0;r<16;r++){ a0[r]=0.f; a1[r]=0.f; }
    u32x4 bc=*LDSP(u32x4,ub+uaddr(-512));
    #pragma unroll
    for(int i=0;i<17;i++){
      u32x4 bn=*LDSP(u32x4,ub+uaddr(-512+32*(i+1)));
      a0=mfma32h(opA[i],bc,a0); a1=mfma32h(opA[i],bn,a1); bc=bn;
    }
    const int ya=S_YD+(ch*2+sub)*YDB+(lane&31)*272+(lane>>5)*16;
    #pragma unroll
    for(int g=0;g<4;g++){ f32x4 v={a0[4*g],a0[4*g+1],a0[4*g+2],a0[4*g+3]}; *LDSP(f32x4,ya+32*g)=v; }
    #pragma unroll
    for(int g=0;g<4;g++){ f32x4 v={a1[4*g],a1[4*g+1],a1[4*g+2],a1[4*g+3]}; *LDSP(f32x4,ya+128+32*g)=v; }
  } else {
    const int rho=lane&15, th=lane>>4;
    const int u1=S_U+ch*UB+uaddr(16*rho+8*th+(sub?-24:-16));
    const int u2=S_U+ch*UB+uaddr(16*rho+8*th+(sub?-520:-16));
    const f32x4 z4={0.f,0.f,0.f,0.f};
    u32x4 X2[6]; u32x4 X3[2]; f32x4 Y[6];
    #pragma unroll
    for(int x=0;x<6;x++){
      u32x4 U=*LDSP(u32x4,u1+576*x), Us=*LDSP(u32x4,u2+576*x);
      f32x4 xa=mfma16h(U,opA[7],z4), xb=mfma16h(U,opA[8],z4);
      X2[x][0]=pkh(xa[0],xa[1]); X2[x][1]=pkh(xa[2],xa[3]); X2[x][2]=pkh(xb[0],xb[1]); X2[x][3]=pkh(xb[2],xb[3]);
      if(x<4){ f32x4 xc=mfma16h(U,opA[9],z4); X3[x>>1][2*(x&1)]=pkh(xc[0],xc[1]); X3[x>>1][2*(x&1)+1]=pkh(xc[2],xc[3]); }
      Y[x]=mfma16h(Us,opA[10],z4);
    }
    #pragma unroll
    for(int x=0;x<6;x++){
      #pragma unroll
      for(int dl=0;dl<3;dl++) if(x-dl>=0) Y[x]=mfma16h(opA[dl],X2[x-dl],Y[x]);
      if(x>=2) Y[x]=mfma16h(opA[3+x-2],X3[0],Y[x]);
      if(x>=4) Y[x]=mfma16h(opA[3+x-4],X3[1],Y[x]);
    }
    const int ya=S_YC+(ch*2+sub)*6144+(64*th+rho)*4;
    #pragma unroll
    for(int x=0;x<6;x++){
      #pragma unroll
      for(int i=0;i<4;i++) *LDSP(float,ya+1024*x+64*i)=Y[x][i];
    }
  }
  STAMP(0,6);
  __syncthreads();
  { const int tbk=tid>>5, tt=(4*tid)&127;
    #pragma unroll
    for(int c=0;c<2;c++){
      const int ya=S_YD+c*2*YDB+(tid>>4)*272+(tid&15)*16;
      f32x4 y=*LDSP(f32x4,ya)+*LDSP(f32x4,ya+YDB);
      if(w>=2){ const int yc=S_YC+c*2*6144+(tid-128)*16; y+=*LDSP(f32x4,yc)+*LDSP(f32x4,yc+6144); }
      float o[4];
      #pragma unroll
      for(int i=0;i<4;i+=2){
        u32 p=pkbf(y[i]*invS[c],y[i+1]*invS[c]);
        u32 q=pkbf(__uint_as_float(p<<16)+ud[c][i],__uint_as_float(p&0xffff0000u)+ud[c][i+1]);
        o[i]=xf[c][i]+__uint_as_float(q<<16); o[i+1]=xf[c][i+1]+__uint_as_float(q&0xffff0000u);
      }
      u32x2 hv={pkbf(o[0],o[1]),pkbf(o[2],o[3])};
      const int dd=d0+c;
      *(u32x2*)(a.ws+OFF_H+(size_t)(tbk*D+dd)*256+(((tt>>3)^hswz(dd))<<4)+(tt&7)*2)=hv;
    }
  }
  STAMP(0,7); STAMP_FLUSH(0);
}

constexpr int G1_STAT=114688;
constexpr int G1_LDS=G1_STAT+2048;
__global__ __launch_bounds__(512,2) void k_g1(const u16* Himg, const u32x4* W1f, u32x4* Act, int SK){
  const int b=blockIdx.x, x=b&7, ii=b>>3;
  const int tb=(x>>1)*4+ii/7, j=7*(x&1)+ii%7;
  const int w=__builtin_amdgcn_readfirstlane(threadIdx.x>>6), lane=threadIdx.x&63;
  STAMP_DECL; STAMP(SK,0);
  { const char* src=(const char*)Himg+(size_t)tb*114688+w*1024+lane*16;
    #pragma unroll
    for(int i=0;i<14;i++) glds16(src+i*8192, w*1024+i*8192); }
  auto rsW=__builtin_amdgcn_make_buffer_rsrc((void*)(W1f+(size_t)(j*8+w)*28*64),0,28*1024,0x00020000);
  u32x4 wf[28];
  #pragma unroll
  for(int s=0;s<28;s++) wf[s]=__builtin_amdgcn_raw_buffer_load_b128(rsW,lane*16,s*1024,0);
  __builtin_amdgcn_sched_barrier(0);
  const int g16=lane>>4, h=g16>>1, th=g16&1, q=(lane>>2)&3, pp=lane&3, wh=w>>2;
  u32 ba[4][2];
  #pragma unroll
  for(int i=0;i<4;i++){ int tau=(i+w)&3;
    #pragma unroll
    for(int e=0;e<2;e++){ int ee=e^wh; int row=8*h+4*ee+q;
      ba[i][e]=row*256+(((4*(tau^q))|((2*th+(pp>>1))^(2*h+ee)))<<4)+8*(pp&1); } }
  f32x16 acc[4];
  #pragma unroll
  for(int i=0;i<4;i++) for(int r=0;r<16;r++) acc[i][r]=0.f;
  float ssum=0.f, ssq=0.f, cs=0.f;
  const u32 ones=0x3f803f80u;
  WAITV(28); BAR();
  STAMP(SK,1);
  #pragma unroll
  for(int s=0;s<28;s++){
    #pragma unroll
    for(int i=0;i<4;i++){
      u32x2 r0=trread(ba[i][0]+s*4096), r1=trread(ba[i][1]+s*4096);
      u32x4 bf={r0[0],r0[1],r1[0],r1[1]};
      if(i==0){ ssum=dot2bf(r0[0],ones,ssum); ssq=dot2bf(r0[0],r0[0],ssq); ssum=dot2bf(r0[1],ones,ssum); ssq=dot2bf(r0[1],r0[1],ssq); }
      acc[i]=mfma32(wf[s],bf,acc[i]);
    }
    cs=dot2bf(wf[s][0],ones,cs); cs=dot2bf(wf[s][1],ones,cs); cs=dot2bf(wf[s][2],ones,cs); cs=dot2bf(wf[s][3],ones,cs);
  }
  STAMP(SK,2);
  ssum+=__shfl_xor(ssum,32); ssq+=__shfl_xor(ssq,32); cs+=__shfl_xor(cs,32);
  if(lane<32){ f32x2 v={ssum,ssq}; *LDSP(f32x2,G1_STAT+(w*32+lane)*8)=v; }
  __syncthreads();
  STAMP(SK,3);
  u32x4 ax={0,0,0,0};
  { float hi=rbf(cs), r1=cs-hi, mid=rbf(r1), lo=r1-mid; if(lane<32){ ax[0]=pkbf(hi,mid); ax[1]=pkbf(lo,0.f);} }
  const float c0=-2.f*0.7978845608028654f*1.4426950408889634f, c1=c0*0.044715f;
  #pragma unroll
  for(int i=0;i<4;i++){
    int tau=(i+w)&3;
    f32x2 a=*LDSP(f32x2,G1_STAT+(tau*32+(lane&31))*8), bb=*LDSP(f32x2,G1_STAT+((tau+4)*32+(lane&31))*8);
    float mean=(a[0]+bb[0])*(1.f/D); float var=(a[1]+bb[1])*(1.f/D)-mean*mean; var=fmaxf(var,0.f);
    float m=rbf(mean); float sd=rbf(__builtin_amdgcn_sqrtf(rbf(rbf(var)+1e-5f)));
    float ri=__builtin_amdgcn_rcpf(sd);
    u32x4 bx={0,0,0,0}; { u32 nm=pkbf(-m,-m); if(lane<32){ bx[0]=nm; bx[1]=nm&0xffffu; } }
    acc[i]=mfma32(ax,bx,acc[i]);
    float k0=c0*ri, k1=c1*ri*ri*ri, s2=sd*sd;
    float o[8];
    #pragma unroll
    for(int r=0;r<8;r++){ float g=acc[i][r], u=acc[i][r+8];
      float z=g*__builtin_fmaf(g*g,k1,k0); float e=__builtin_amdgcn_exp2f(z); float rr=__builtin_amdgcn_rcpf(__builtin_fmaf(e,s2,s2));
      o[r]=(g*u)*rr; }
    u32x4 ov={pkbf(o[0],o[1]),pkbf(o[2],o[3]),pkbf(o[4],o[5]),pkbf(o[6],o[7])};
    if(i==0) STAMP(SK,4);
    Act[((size_t)((tb*4+tau)*112+j*8+w))*64+lane]=ov;
    if(i==0) STAMP(SK,5);
    if(i==3) STAMP(SK,6);
  }
  WAITV(0);
  STAMP(SK,7); STAMP_FLUSH(SK);
}

constexpr int G2_LDS=160*1024;
__global__ __launch_bounds__(512,2) void k_g2(const u16* Himg, const u32x4* Wdf, const u32x4* Act, u16* out){
  const int b=blockIdx.x, x=b&7, ii=b>>3;
  const int tb=(x>>1)*4+ii/7, half=x&1, jj=ii%7;
  const int w=__builtin_amdgcn_readfirstlane(threadIdx.x>>6), lane=threadIdx.x&63;
  const int dsel=w&1, q=w>>1;
  STAMP_DECL; STAMP(2,0);
  const char* asrc=(const char*)(Act+((size_t)((tb*4+half*2+dsel)*112+q*28))*64+lane);
  #pragma unroll
  for(int c=0;c<5;c++){
    #pragma unroll
    for(int k=0;k<4;k++) glds16(asrc+(c*4+k)*1024, c*32768+w*4096+k*1024);
  }
  auto rsW=__builtin_amdgcn_make_buffer_rsrc((void*)(Wdf+(size_t)((2*jj+dsel)*112+q*28)*64),0,28*1024,0x00020000);
  u32x4 wf[28];
  #pragma unroll
  for(int s=0;s<28;s++) wf[s]=__builtin_amdgcn_raw_buffer_load_b128(rsW,lane*16,s*1024,0);
  u16 hres[8];
  { int tt=(half*2+(w&1))*32+(lane&31); int d0=jj*64+(w>>1)*16+(lane>>5)*8;
    #pragma unroll
    for(int e=0;e<8;e++){ int d=d0+e; hres[e]=Himg[((size_t)(tb*D+d))*128+(((tt>>3)^hswz(d))<<3)+(tt&7)]; } }
  __builtin_amdgcn_sched_barrier(0);
  f32x16 acc[2];
  #pragma unroll
  for(int i=0;i<2;i++) for(int r=0;r<16;r++) acc[i][r]=0.f;
  WAITV(0); BAR();
  STAMP(2,1);
  #pragma unroll
  for(int c=0;c<7;c++){
    if(c==5){ WAITV(0); BAR(); }
    int slot=(c%5)*32768+q*8192+lane*16;
    #pragma unroll
    for(int k=0;k<4;k++){
      #pragma unroll
      for(int i=0;i<2;i++){ u32x4 bf=*LDSP(u32x4,slot+i*4096+k*1024); acc[i]=mfma32(wf[c*4+k],bf,acc[i]); }
    }
    if(c==1){ WAITL(0); BAR();
      #pragma unroll
      for(int c2=5;c2<7;c2++){
        #pragma unroll
        for(int k=0;k<4;k++) glds16(asrc+(c2*4+k)*1024, (c2-5)*32768+w*4096+k*1024);
      } }
  }
  STAMP(2,2);
  WAITL(0); BAR();
  constexpr int RS=272;
  { int tok=lane&31, hh=lane>>5;
    #pragma unroll
    for(int i=0;i<2;i++){
      #pragma unroll
      for(int g=0;g<4;g++){ f32x4 v={acc[i][4*g],acc[i][4*g+1],acc[i][4*g+2],acc[i][4*g+3]};
        *LDSP(f32x4,((q*2+i)*32+tok)*RS+(dsel*32+8*g+4*hh)*4)=v; } } }
  __syncthreads();
  { int tok=lane&31, hh=lane>>5; int i=w&1; int dl=(w>>1)*16+hh*8;
    f32x4 s0={0,0,0,0}, s1={0,0,0,0};
    #pragma unroll
    for(int qq=0;qq<4;qq++){ int a=((qq*2+i)*32+tok)*RS+dl*4; s0+=*LDSP(f32x4,a); s1+=*LDSP(f32x4,a+16); }
    float o[8]={s0[0],s0[1],s0[2],s0[3],s1[0],s1[1],s1[2],s1[3]};
    #pragma unroll
    for(int e=0;e<8;e++) o[e]=bf2f(hres[e])+rbf(o[e]);
    u32x4 ov={pkbf(o[0],o[1]),pkbf(o[2],o[3]),pkbf(o[4],o[5]),pkbf(o[6],o[7])};
    int t=tb*128+(half*2+i)*32+tok;
    *(u32x4*)(out+(size_t)t*D+jj*64+dl)=ov;
  }
  STAMP(2,3);
  WAITV(0);
  STAMP(2,4); STAMP_FLUSH(2);
}

extern "C" void kernel_launch(void* const* d_in, const int* in_sizes, int n_in,
                              void* d_out, int out_size, void* d_ws, size_t ws_size,
                              hipStream_t stream) {
  char* ws=(char*)d_ws;
  u16* Himg=(u16*)(ws+OFF_H); u32x4* W1f=(u32x4*)(ws+OFF_W1); u32x4* Wdf=(u32x4*)(ws+OFF_WD); u32x4* Act=(u32x4*)(ws+OFF_ACT);
  (void)hipFuncSetAttribute((const void*)k_g1,hipFuncAttributeMaxDynamicSharedMemorySize,G1_LDS);
  (void)hipFuncSetAttribute((const void*)k_g2,hipFuncAttributeMaxDynamicSharedMemorySize,G2_LDS);
  SArgs sa;
  sa.x=(const u16*)d_in[0]; sa.Are=(const u16*)d_in[1]; sa.Aim=(const u16*)d_in[2]; sa.Bre=(const u16*)d_in[3]; sa.Bim=(const u16*)d_in[4];
  sa.Cre=(const u16*)d_in[5]; sa.Cim=(const u16*)d_in[6]; sa.logdt=(const u16*)d_in[7]; sa.Dskip=(const u16*)d_in[8];
  sa.wg=(const u16*)d_in[10]; sa.wu=(const u16*)d_in[11]; sa.wd=(const u16*)d_in[12]; sa.ws=ws;
  k_s<<<256,512,0,stream>>>(sa);
  k_g1<<<224,512,G1_LDS,stream>>>(Himg,W1f,Act,1);
  k_g2<<<224,512,G2_LDS,stream>>>(Himg,Wdf,Act,(u16*)d_out);
}
```

```cpp
#include <hip/hip_runtime.h>
#include <stdint.h>
typedef unsigned short u16; typedef unsigned u32; typedef unsigned long long u64;
typedef __attribute__((ext_vector_type(4))) unsigned u32x4;
typedef __attribute__((ext_vector_type(2))) unsigned u32x2;
typedef __attribute__((ext_vector_type(4))) short s16x4;
typedef __attribute__((ext_vector_type(2))) __bf16 bf16x2;
typedef __attribute__((ext_vector_type(8))) __bf16 bf16x8;
typedef __attribute__((ext_vector_type(2))) _Float16 f16x2;
typedef __attribute__((ext_vector_type(8))) _Float16 f16x8;
typedef __attribute__((ext_vector_type(2))) float f32x2;
typedef __attribute__((ext_vector_type(4))) float f32x4;
typedef __attribute__((ext_vector_type(16))) float f32x16;
#define DI __device__ __forceinline__
#define LDSP(T, off) ((__attribute__((address_space(3))) T*)(uintptr_t)(u32)(off))
#define GAS __attribute__((address_space(1)))
constexpr int L=2048, D=448, N=256, F=1792;

DI float bf2f(u16 v){ return __uint_as_float(((unsigned)v)<<16); }
DI float lo16(u32 v){ return __uint_as_float(v<<16); }
DI float hi16(u32 v){ return __uint_as_float(v&0xffff0000u); }
DI u32 pkbf(float a,float b){ f32x2 v={a,b}; return __builtin_bit_cast(u32,__builtin_convertvector(v,bf16x2)); }
DI u32 pkbf2(f32x2 v){ return __builtin_bit_cast(u32,__builtin_convertvector(v,bf16x2)); }
DI float rbf(float f){ return lo16(pkbf(f,f)); }
DI u32 pkh(float a,float b){ f32x2 v={a,b}; return __builtin_bit_cast(u32,__builtin_convertvector(v,f16x2)); }
DI u32 pkh2(f32x2 v){ return __builtin_bit_cast(u32,__builtin_convertvector(v,f16x2)); }
DI f32x16 mfma32(u32x4 a,u32x4 b,f32x16 c){ return __builtin_amdgcn_mfma_f32_32x32x16_bf16(__builtin_bit_cast(bf16x8,a),__builtin_bit_cast(bf16x8,b),c,0,0,0); }
DI f32x16 mfma32h(u32x4 a,u32x4 b,f32x16 c){ return __builtin_amdgcn_mfma_f32_32x32x16_f16(__builtin_bit_cast(f16x8,a),__builtin_bit_cast(f16x8,b),c,0,0,0); }
DI f32x4 mfma16h(u32x4 a,u32x4 b,f32x4 c){ return __builtin_amdgcn_mfma_f32_16x16x32_f16(__builtin_bit_cast(f16x8,a),__builtin_bit_cast(f16x8,b),c,0,0,0); }
DI u32x2 trread(u32 addr){ return __builtin_bit_cast(u32x2,__builtin_amdgcn_ds_read_tr16_b64_v4i16(LDSP(s16x4,addr))); }
DI u32x4 mkrs(const void* p,u32 bytes){ u64 a=(u64)p; u32x4 r={(u32)a,(u32)(a>>32)&0xffffu,bytes,0x00020000u}; return r; }
DI void glds16(const void* gsrc, u32 lds_dst){
  asm volatile("s_mov_b32 m0, %1\n\ts_nop 0\n\tglobal_load_lds_dwordx4 %0, off" :: "v"(gsrc), "s"(lds_dst) : "memory");
}
template<int OFF> DI void blds16(u32 voff,u32x4 rs,u32 soff,u32 lds_dst){
  asm volatile("s_mov_b32 m0, %3\n\ts_nop 0\n\tbuffer_load_dwordx4 %0, %1, %2 offen offset:%4 lds" :: "v"(voff), "s"(rs), "s"(soff), "s"(lds_dst), "i"(OFF) : "memory");
}
template<int OFF> DI void blds16_sc1(u32 voff,u32x4 rs,u32 soff,u32 lds_dst){
  asm volatile("s_mov_b32 m0, %3\n\ts_nop 0\n\tbuffer_load_dwordx4 %0, %1, %2 offen offset:%4 sc1 lds" :: "v"(voff), "s"(rs), "s"(soff), "s"(lds_dst), "i"(OFF) : "memory");
}
template<int OFF> DI void bld16(u32x4& dst,u32 voff,u32x4 rs,u32 soff){
  asm volatile("buffer_load_dwordx4 %0, %1, %2, %3 offen offset:%4" : "=v"(dst) : "v"(voff), "s"(rs), "s"(soff), "i"(OFF));
}
template<int NN> DI void waitv(){ asm volatile("s_waitcnt vmcnt(%0)" :: "i"(NN) : "memory"); }
template<int NN> DI void waitw(u32x4& r){ asm volatile("s_waitcnt vmcnt(%1)" : "+v"(r) : "i"(NN)); }
#define WAITL0() asm volatile("s_waitcnt lgkmcnt(0)" ::: "memory")
#define BAR() __builtin_amdgcn_s_barrier()
#define SB __builtin_amdgcn_sched_barrier(0)
#define LSYNC() do{ WAITL0(); BAR(); asm volatile("" ::: "memory"); }while(0)

#define STAMP_DECL
#define STAMP_FLUSH(k)
#define STAMP(k,i)

constexpr size_t OFF_H   = 0;
constexpr size_t OFF_W1  = 2u<<20;
constexpr size_t OFF_WD  = 6u<<20;
constexpr size_t OFF_ACT = 8u<<20;
constexpr size_t OFF_GR  = 16u<<20;
constexpr size_t OFF_EP  = OFF_GR+16384;
constexpr size_t OFF_G2  = OFF_GR+32768;
constexpr size_t OFF_CNT = OFF_G2+16384;
constexpr size_t OFF_XG  = 17u<<20;
DI int hswz(int d){ return ((d&3)<<2)|((d>>2)&3); }

template<int CTRL,int RM> DI float dppf(float v){ return __builtin_bit_cast(float,__builtin_amdgcn_update_dpp(0,__builtin_bit_cast(int,v),CTRL,RM,0xf,false)); }
DI float wave_sum(float v){
  v+=dppf<0x111,0xf>(v); v+=dppf<0x112,0xf>(v); v+=dppf<0x114,0xf>(v); v+=dppf<0x118,0xf>(v);
  v+=dppf<0x142,0xa>(v); v+=dppf<0x143,0xc>(v);
  return __builtin_bit_cast(float,__builtin_amdgcn_readlane(__builtin_bit_cast(int,v),63));
}
DI float wave_max_pos(float v){
  v=fmaxf(v,dppf<0x111,0xf>(v)); v=fmaxf(v,dppf<0x112,0xf>(v)); v=fmaxf(v,dppf<0x114,0xf>(v)); v=fmaxf(v,dppf<0x118,0xf>(v));
  v=fmaxf(v,dppf<0x142,0xa>(v)); v=fmaxf(v,dppf<0x143,0xc>(v));
  return __builtin_bit_cast(float,__builtin_amdgcn_readlane(__builtin_bit_cast(int,v),63));
}

DI int pswz(int row){ return (((row>>1)&1)<<1)|((row>>6)&1); }
DI void prep_load(int u, u32 lb, const u16* wg, const u16* wu, const u16* wd, int w, int lane){
  #pragma unroll
  for(int hf=0;hf<2;hf++){
    const int row=64*hf+8*w+(lane>>3), ch=(lane&7)^(pswz(row)<<1);
    const u16* src;
    if(u<196){ int kc=u/28, fc=u%28; src=(hf?wu:wg)+(size_t)(kc*64+(row&63))*F+fc*64+ch*8; }
    else { int v=u-196; int fc=v/7, dc=v%7; src=wd+(size_t)(fc*128+row)*D+dc*64+ch*8; }
    glds16(src, lb+hf*8192+w*1024);
  }
}
DI void prep_emit(int u, u32 lb, char* ws, int w4, int lane){
  u32x4* W1f=(u32x4*)(ws+OFF_W1); u32x4* Wdf=(u32x4*)(ws+OFF_WD);
  const int g16=lane>>4, h=g16>>1, sub=g16&1, q=(lane>>2)&3, pp=lane&3;
  if(u<196){ int kc=u/28, fc=u%28; int jw=(fc>>1)*8+(fc&1)*4+w4;
    const u32x4 ones={0x3f803f80u,0x3f803f80u,0x3f803f80u,0x3f803f80u};
    f32x16 cs; for(int r=0;r<16;r++) cs[r]=0.f;
    u32x4* dst=W1f+(size_t)(jw*29)*64;
    #pragma unroll
    for(int s=0;s<4;s++){ u32x2 r[2];
      #pragma unroll
      for(int e=0;e<2;e++){ int rr=sub*64+s*16+8*h+4*e+q; int c=w4*2+(pp>>1); r[e]=trread(lb+rr*128+((c^(pswz(rr)<<1))<<4)+8*(pp&1)); }
      u32x4 o={r[0][0],r[0][1],r[1][0],r[1][1]};
      dst[(4*kc+s)*64+lane]=o;
      cs=mfma32(ones,o,cs);
    }
    float hi=rbf(cs[0]); u32 pk=pkbf(hi,cs[0]-hi);
    u32* cd=(u32*)(dst+28*64+(lane&31)+32*(kc>>2))+(kc&3);
    if(lane<32) cd[0]=pk; else if(kc==6) cd[1]=0u;
  } else { int v=u-196; int fc=v/7, dc=v%7;
    #pragma unroll
    for(int s=0;s<4;s++){ u32x2 r[2]; const int sg=w4*2+(s>>1), dl=s&1;
      #pragma unroll
      for(int e=0;e<2;e++){ int rr=sg*16+8*e+4*h+q; int c=dl*4+sub*2+(pp>>1); r[e]=trread(lb+rr*128+((c^(pswz(rr)<<1))<<4)+8*(pp&1)); }
      u32x4 o={r[0][0],r[0][1],r[1][0],r[1][1]};
      Wdf[((size_t)((2*dc+dl)*112+8*fc+sg))*64+lane]=o;
    }
  }
}

typedef f32x2 cf;
struct cm{ cf a,b; };
DI cm mkm(cf w){ cm m; m.a=w; m.b=cf{-w[1],w[0]}; return m; }
DI cf cmul(cf x,cm m){ cf xx={x[0],x[0]}, yy={x[1],x[1]}; return __builtin_elementwise_fma(yy,m.b,xx*m.a); }
DI cf cexpz(float a2,float br,float l){ float e=__builtin_amdgcn_exp2f(a2*l); float p=__builtin_amdgcn_fractf(br*l); cf r={__builtin_amdgcn_cosf(p),__builtin_amdgcn_sinf(p)}; return r*e; }

constexpr int TS=2064;
constexpr int S_U   = 53*TS;
constexpr int UPAD=576, UB=42*144;
constexpr int S_KR  = S_U+2*UB;
constexpr int KRB=1216;
constexpr int S_CT  = S_KR+4*KRB;
constexpr int CTB=192;
constexpr int S_EX  = S_CT+2*6*2*CTB;
constexpr int S_XB  = S_EX+128;
constexpr int XS=912;
constexpr int S_PW  = S_XB+8*XS;
constexpr int S_END = S_PW+16384;
constexpr int YDB=32*272;
constexpr int S_YD=0, S_YC=4*YDB;
constexpr int S_LDS = 160*1024;
static_assert(S_END<=S_LDS && (S_PW&15)==0 && (S_U&15)==0 && (S_XB&15)==0,"lds");
static_assert(S_YC+4*6144<=S_U,"lds");
DI constexpr int uaddr(int s){ return 2*(s+UPAD)+16*((s+UPAD)>>6); }
template<int OFF> DI void put_tid(u32 v){ asm volatile("ds_write_addtid_b32 %0 offset:%1" :: "v"(v), "i"(OFF) : "memory"); }
DI void set_m0(u32 v){ asm volatile("s_mov_b32 m0, %0\n\ts_nop 0" :: "s"(v)); }

struct SArgs{ const u16 *x,*Are,*Aim,*Bre,*Bim,*Cre,*Cim,*logdt,*Dskip,*wg,*wu,*wd; char* ws; };

__global__ __launch_bounds__(512,2) void k_s(SArgs a){
  __shared__ __attribute__((aligned(16))) char smem[S_LDS];
  asm volatile("" :: "v"(smem));
  STAMP_DECL; STAMP(0,0);
  const int b=blockIdx.x, tid=threadIdx.x;
  const int w=__builtin_amdgcn_readfirstlane(tid>>6), lane=tid&63;
  auto rsX=__builtin_amdgcn_make_buffer_rsrc((void*)a.x,0,L*D*2,0x00020000);
  auto rsG=__builtin_amdgcn_make_buffer_rsrc((void*)(a.ws+OFF_GR),0,L*8,0x00020000);
  auto rsT=__builtin_amdgcn_make_buffer_rsrc((void*)(a.ws+OFF_XG),0,224*L*8,0x00020000);
  const int row=b*8+w;
  u32x4 xr=__builtin_amdgcn_raw_buffer_load_b128(rsX,lane<56?lane*16:0x7ffffff0,row*(D*2),0);
  u32 epoch=__hip_atomic_load((GAS u32*)(a.ws+OFF_EP),__ATOMIC_RELAXED,__HIP_MEMORY_SCOPE_AGENT);
  const bool isS=b<224;
  const int pr=isS?b:0, d0=2*pr;
  const int ch=w>>2, n=tid&255, d=d0+ch;
  const int po=d*N+n;
  const u16 pAr=a.Are[po], pAi=a.Aim[po], pBr=a.Bre[po], pBi=a.Bim[po], pCr=a.Cre[po], pCi=a.Cim[po], pdt=a.logdt[d];
  const u32 pD=*(const u32*)(a.Dskip+d0);
  SB;
  { u32x4 z={0,0,0,0};
    #pragma unroll
    for(int i=0;i<3;i++){ int o2=S_U+tid*16+i*8192; if(o2<S_EX) *LDSP(u32x4,o2)=z; } }
  const bool isD=((w&2)==0)==(ch==0);
  const int sub=w&1;
  u32 ca0,ca1,ca1s,ca2,cb1,cb2;
  { const int kr=S_KR+ch*2*KRB, ct=S_CT+ch*12*CTB, r=n>>5, j=n&31, dump=S_EX+64;
    ca0=kr+2*(544-(r+8*j));
    if(r<4){ int v=256+2*r+8*j; ca1=kr+2*(544-v); cb1=KRB+2; ca1s=(r&1)?dump+4:ca1; }
    else { int i2=64*((r-4)>>1)+(r&1)+2*j; int ph=(i2-1)&3, q2=(i2-1)>>2;
      ca1=ct+((ph>>1)*3+(ph&1))*2*CTB+2*(64-q2); cb1=CTB+2; ca1s=dump+4;
      if(i2==0){ ca1=kr+2*(544-511); cb1=KRB+2; } }
    { int ps=(n-1)&1, q3=(n-1)>>1; ca2=ct+(ps*3+2)*2*CTB+2*(64-q3); cb2=CTB+2; if(n==0){ ca2=S_EX+32+ch*4+2; cb2=0; } }
  }
  u32x4 band[4];
  if(!isD){
    const int rho=lane&15, th=lane>>4, o=sub?-24:-16;
    #pragma unroll
    for(int ty=0;ty<4;ty++){
      int off,lo,hi;
      if(ty==0){ off=o+(sub?12:4); lo=-1; hi=1; }
      else if(ty==1){ off=o+(sub?16:8); lo=-2; hi=2; }
      else if(ty==2){ off=o+(sub?16:8); lo=sub?-4:-3; hi=sub?4:3; }
      else { off=sub?-8:-16; lo=sub?-4:-2; hi=sub?2:0; }
      const u32 one=(ty==3)?0xffffu:0x3c00u;
      #pragma unroll
      for(int e2=0;e2<4;e2++){
        int p0=8*th+2*e2+off-rho;
        u32 v0=(u32)(p0-lo)<=(u32)(hi-lo)?one:0u, v1=(u32)(p0+1-lo)<=(u32)(hi-lo)?(one<<16):0u;
        band[ty][e2]=v0|v1;
      }
    }
  } else { for(int ty=0;ty<4;ty++) band[ty]=u32x4{0,0,0,0}; }
  SB;
  if(lane<56) *LDSP(u32x4,S_XB+w*XS+lane*16)=xr;
  const u32 tag=__builtin_amdgcn_readfirstlane(epoch)+1u;
  { f32x2 v[4];
    #pragma unroll
    for(int i=0;i<4;i++){ v[i]=f32x2{lo16(xr[i]),hi16(xr[i])}; }
    f32x2 s2=(v[0]+v[1])+(v[2]+v[3]);
    float mean=wave_sum(s2[0]+s2[1])*(1.f/D);
    f32x2 m2={mean,mean}, q2={0.f,0.f};
    #pragma unroll
    for(int i=0;i<4;i++){ f32x2 c=v[i]-m2; q2=__builtin_elementwise_fma(c,c,q2); }
    float q=lane<56?q2[0]+q2[1]:0.f;
    float var=wave_sum(q)*(1.f/D);
    float sd=__builtin_amdgcn_sqrtf(rbf(rbf(var)+1e-5f));
    if(lane==0){ u64 g=((u64)tag<<32)|pkbf(mean,sd); __hip_atomic_store((GAS u64*)(a.ws+OFF_GR)+row,g,__ATOMIC_RELAXED,__HIP_MEMORY_SCOPE_AGENT); }
  }
  STAMP(0,1);
  auto xpublish=[&](){
    LSYNC();
    #pragma unroll
    for(int i=0;i<2;i++){ const int id=tid+512*i;
      if(i==0||tid<384){ const int p=id>>2, tq=id&3; const u32 ra=S_XB+2*tq*XS+p*4;
        u32x4 v={*LDSP(u32,ra),tag,*LDSP(u32,ra+XS),tag};
        __builtin_amdgcn_raw_buffer_store_b128(v,rsT,p*(L*8)+tq*16,b*64,16); } }
  };
  if(!isS){
    const int p=b-224;
    xpublish();
    __builtin_amdgcn_s_sleep(24);
    #pragma unroll 1
    for(int it=0;it<3;it++){ int u=224+p+32*it; if(u<294) prep_load(u,it*16384,a.wg,a.wu,a.wd,w,lane); }
    if(tid<40){ u32x4 z={0,0,0,0}; *(u32x4*)(a.ws+OFF_G2+(p*40+tid)*16)=z; }
    waitv<0>(); BAR();
    #pragma unroll 1
    for(int it=(w>>2);it<3;it+=2){ int u=224+p+32*it; if(u<294) prep_emit(u,it*16384,a.ws,w&3,lane); }
    STAMP(0,11); STAMP_FLUSH(0);
    return;
  }
  { float ld=bf2f(pdt); float ph=ld*1.4426950408889634f; float pl=__builtin_fmaf(ld,1.4426950408889634f,-ph)+ld*1.9259629911e-8f;
    float e1=__builtin_amdgcn_exp2f(ph); float dt=rbf(__builtin_fmaf(e1*pl,0.6931471805599453f,e1));
    float a2=(dt*bf2f(pAr))*1.4426950408889634f, br=(dt*bf2f(pAi))*0.15915494309189535f;
    cf B={dt*bf2f(pBr),dt*bf2f(pBi)}, C={bf2f(pCr),bf2f(pCi)};
    cf cb=cmul(C,mkm(B));
    float g=fmaxf(a2,0.f);
    float eg=__builtin_amdgcn_exp2f(248.f*g), e0=__builtin_amdgcn_exp2f(-248.f*g);
    float mag=(fabsf(cb[0])+fabsf(cb[1]))*eg*__builtin_amdgcn_exp2f(2048.f*g);
    u32 ex=__float_as_uint(wave_max_pos(mag))>>23; ex=ex<6u?6u:(ex>254u?254u:ex);
    float sig=__uint_as_float((259u-ex)<<23), isig=__uint_as_float((ex-5u)<<23);
    if(lane==0) *LDSP(u32,S_EX+w*4)=ex;
    cb*=eg*sig;
    cf v1=cexpz(a2,br,1.f), w8=cexpz(a2,-br,8.f);
    cm G=mkm(cexpz(a2,br,256.f));
    #define PUT(r,val) put_tid<(r)*TS>(pkh2(val))
    STAMP(0,2);
    set_m0(w*256+21*TS);
    { cm w16=mkm(cmul(w8,mkm(w8))); cf ea={e0,0.f}; cf eb=w8*e0;
      #define ESTEP(j) PUT(2*(j),ea); PUT(2*(j)+1,eb); if((j)<15){ ea=cmul(ea,w16); eb=cmul(eb,w16);}
      ESTEP(0) ESTEP(1) ESTEP(2) ESTEP(3) ESTEP(4) ESTEP(5) ESTEP(6) ESTEP(7) ESTEP(8) ESTEP(9) ESTEP(10) ESTEP(11) ESTEP(12) ESTEP(13) ESTEP(14) ESTEP(15) }
    STAMP(0,3);
    xpublish();
    prep_load(b,S_PW,a.wg,a.wu,a.wd,w,lane);
    STAMP(0,4);
    const u32 fa=w*256+(lane&31)*TS+(lane>>5)*16;
    u32x4 Ef[8];
    #pragma unroll
    for(int s=0;s<8;s++) Ef[s]=*LDSP(u32x4,fa+21*TS+32*s);
    set_m0(w*256);
    { cm m1=mkm(v1); cf v2=cmul(v1,m1); cm m2=mkm(v2); cm m4=mkm(cmul(v2,m2));
      cf x0=cb, x1=cmul(cb,m1), x2=cmul(x0,m2), x3=cmul(x1,m2), x4=cmul(x0,m4), x5=cmul(x1,m4), x6=cmul(x2,m4), x7=cmul(x3,m4);
      PUT(0,x0);PUT(1,x1);PUT(2,x2);PUT(3,x3);PUT(4,x4);PUT(5,x5);PUT(6,x6);PUT(7,x7);
      cf g2=cmul(G.a,G); cm G2=mkm(g2); cm G4=mkm(cmul(g2,G2));
      cf y1=cmul(x0,G), z1=cmul(x4,G), y2=cmul(x0,G2), y4=cmul(x0,G4);
      PUT(8,y1); PUT(9,cmul(x2,G)); PUT(10,z1); PUT(11,cmul(x6,G));
      cf y3=cmul(y1,G2);
      PUT(12,y2); PUT(13,cmul(x4,G2)); PUT(14,y3); PUT(15,cmul(z1,G2));
      PUT(16,y4); PUT(17,cmul(y1,G4)); PUT(18,cmul(y2,G4)); PUT(19,cmul(y3,G4)); PUT(20,cmul(y4,G4)); }
    f32x16 acc; for(int r=0;r<16;r++) acc[r]=0.f;
    #pragma unroll
    for(int s=0;s<8;s++){ u32x4 A=*LDSP(u32x4,fa+32*s); acc=mfma32h(A,Ef[s],acc); }
    const u32 pa=w*256+(lane>>5)*4*TS+(lane&31)*4;
    #pragma unroll
    for(int r=0;r<12;r++){ int rr=(r&3)+8*(r>>2); *LDSP(float,pa+rr*TS)=acc[r]*isig; }
  }
  STAMP(0,5);
  u32x4 g0,g1,x0,x1;
  #define POLL() do{ g0=__builtin_amdgcn_raw_buffer_load_b128(rsG,tid*32,0,16); g1=__builtin_amdgcn_raw_buffer_load_b128(rsG,tid*32+16,0,16); \
    x0=__builtin_amdgcn_raw_buffer_load_b128(rsT,tid*32,pr*(L*8),16); x1=__builtin_amdgcn_raw_buffer_load_b128(rsT,tid*32+16,pr*(L*8),16); }while(0)
  POLL();
  STAMP(0,6);
  LSYNC();
  float invS[2];
  { u32x4 e0=*LDSP(u32x4,S_EX), e1=*LDSP(u32x4,S_EX+16);
    const u32 ra=ch*1024+(n>>5)*TS+(n&31)*4;
    float val[3];
    #pragma unroll
    for(int it=0;it<3;it++){ const u32 r_=ra+it*8*TS; val[it]=(*LDSP(float,r_)+*LDSP(float,r_+256))+(*LDSP(float,r_+512)+*LDSP(float,r_+768)); }
    u32 m0=max(max(e0[0],e0[1]),max(e0[2],e0[3])), m1=max(max(e1[0],e1[1]),max(e1[2],e1[3]));
    invS[0]=__uint_as_float((m0-5u)<<23); invS[1]=__uint_as_float((m1-5u)<<23);
    float SK=__uint_as_float((259u-(ch?m1:m0))<<23);
    _Float16 h0=(_Float16)(val[0]*SK), h1=(_Float16)(val[1]*SK), h2=(_Float16)(val[2]*SK);
    if((w&3)<2){
      *LDSP(_Float16,ca1s-2)=h1; *LDSP(_Float16,ca1s+(ca1s==ca1?KRB:0))=h1;
      if(n!=0){ *LDSP(_Float16,ca1s+2)=h1; *LDSP(_Float16,ca1s+4+(ca1s==ca1?KRB:0))=h1; }
    }
    *LDSP(_Float16,ca0)=h0; *LDSP(_Float16,ca0+KRB+2)=h0;
    *LDSP(_Float16,ca1)=h1; *LDSP(_Float16,ca1+cb1)=h1;
    if(n==128) *LDSP(_Float16,S_EX+32+ch*4)=h1;
    if(n<=128){ *LDSP(_Float16,ca2)=h2; *LDSP(_Float16,ca2+cb2)=h2; }
  }
  f32x2 xf[4], ud[4];
  { int spins=0;
    while(true){
      bool ok=(g0[1]==tag)&&(g0[3]==tag)&&(g1[1]==tag)&&(g1[3]==tag)&&(x0[1]==tag)&&(x0[3]==tag)&&(x1[1]==tag)&&(x1[3]==tag);
      if(__all(ok)||++spins>(1<<16)) break;
      __builtin_amdgcn_s_sleep(1);
      asm volatile("" ::: "memory");
      POLL();
    }
    STAMP(0,7);
    const u32 st[4]={g0[0],g0[2],g1[0],g1[2]}, xg[4]={x0[0],x0[2],x1[0],x1[2]};
    f32x2 uu[4];
    const f32x2 D2={lo16(pD),hi16(pD)};
    #pragma unroll
    for(int i=0;i<4;i++){
      float m=lo16(st[i]), ri=__builtin_amdgcn_rcpf(hi16(st[i]));
      xf[i]=f32x2{lo16(xg[i]),hi16(xg[i])};
      u32 c=pkbf2(xf[i]-f32x2{m,m});
      u32 un=pkbf2(f32x2{lo16(c),hi16(c)}*f32x2{ri,ri});
      uu[i]=f32x2{lo16(un),hi16(un)};
    }
    const u32 ua=S_U+uaddr(4*tid);
    #pragma unroll
    for(int c=0;c<2;c++){ u32x2 v={pkh(uu[0][c],uu[1][c]),pkh(uu[2][c],uu[3][c])}; *LDSP(u32x2,ua+c*UB)=v; }
    waitv<0>();
    LSYNC();
    #pragma unroll
    for(int i=0;i<4;i++){ u32 q=pkbf2(uu[i]*D2); ud[i]=f32x2{lo16(q),hi16(q)}; }
  }
  if(b==0&&tid==0) __hip_atomic_store((GAS u32*)(a.ws+OFF_EP),tag,__ATOMIC_RELAXED,__HIP_MEMORY_SCOPE_AGENT);
  STAMP(0,8);
  if(isD){
    const u32 ka=S_KR+ch*2*KRB+(lane&1)*(KRB+2)+2*(32-(lane&31)+8*(lane>>5)+16*sub);
    const u32 ub=S_U+ch*UB+144*(lane&31)+16*(lane>>5)+32*sub;
    f32x16 a0,a1; for(int r=0;r<16;r++){ a0[r]=0.f; a1[r]=0.f; }
    u32x4 bc=*LDSP(u32x4,ub+uaddr(-512));
    #pragma unroll
    for(int i=0;i<17;i++){
      u32x4 A={*LDSP(u32,ka+64*i),*LDSP(u32,ka+64*i+4),*LDSP(u32,ka+64*i+8),*LDSP(u32,ka+64*i+12)};
      u32x4 bn=*LDSP(u32x4,ub+uaddr(-512+32*(i+1)));
      a0=mfma32h(A,bc,a0); a1=mfma32h(A,bn,a1); bc=bn;
    }
    const u32 ya=S_YD+(ch*2+sub)*YDB+(lane&31)*272+(lane>>5)*16;
    #pragma unroll
    for(int g=0;g<4;g++){ f32x4 v={a0[4*g],a0[4*g+1],a0[4*g+2],a0[4*g+3]}; *LDSP(f32x4,ya+32*g)=v; }
    #pragma unroll
    for(int g=0;g<4;g++){ f32x4 v={a1[4*g],a1[4*g+1],a1[4*g+2],a1[4*g+3]}; *LDSP(f32x4,ya+128+32*g)=v; }
  } else {
    const int rho=lane&15, th=lane>>4;
    const u32 ca=S_CT+ch*12*CTB+sub*6*CTB+(rho&1)*(CTB+2)+2*(64-rho+4*th)-96;
    const u32 u1=S_U+ch*UB+uaddr(16*rho+8*th+(sub?-24:-16));
    const u32 u2=S_U+ch*UB+uaddr(16*rho+8*th+(sub?-520:-16));
    u32x4 T2[3], T3[4];
    #pragma unroll
    for(int dl=0;dl<3;dl++) T2[dl]=u32x4{*LDSP(u32,ca+96-32*dl),*LDSP(u32,ca+96-32*dl+4),*LDSP(u32,ca+96+2*CTB-32*dl),*LDSP(u32,ca+96+2*CTB-32*dl+4)};
    #pragma unroll
    for(int dl=0;dl<4;dl++){
      T3[dl][0]=*LDSP(u32,ca+96+4*CTB-32*dl); T3[dl][1]=*LDSP(u32,ca+96+4*CTB-32*dl+4);
      if(dl==0){ T3[0][2]=0; T3[0][3]=0; } else { T3[dl][2]=T3[dl-1][0]; T3[dl][3]=T3[dl-1][1]; }
    }
    { u32 sp=*LDSP(u16,S_EX+32+ch*4+2*sub); sp|=sp<<16; band[3]&=u32x4{sp,sp,sp,sp}; }
    const f32x4 z4={0.f,0.f,0.f,0.f};
    u32x4 X2[6]; u32x4 X3[2]; f32x4 Y[6];
    #pragma unroll
    for(int x=0;x<6;x++){
      u32x4 U=*LDSP(u32x4,u1+576*x), Us=*LDSP(u32x4,u2+576*x);
      f32x4 xa=mfma16h(U,band[0],z4), xb=mfma16h(U,band[1],z4);
      X2[x][0]=pkh(xa[0],xa[1]); X2[x][1]=pkh(xa[2],xa[3]); X2[x][2]=pkh(xb[0],xb[1]); X2[x][3]=pkh(xb[2],xb[3]);
      if(x<4){ f32x4 xc=mfma16h(U,band[2],z4); X3[x>>1][2*(x&1)]=pkh(xc[0],xc[1]); X3[x>>1][2*(x&1)+1]=pkh(xc[2],xc[3]); }
      Y[x]=mfma16h(Us,band[3],z4);
    }
    #pragma unroll
    for(int x=0;x<6;x++){
      #pragma unroll
      for(int dl=0;dl<3;dl++) if(x-dl>=0) Y[x]=mfma16h(T2[dl],X2[x-dl],Y[x]);
      if(x>=2) Y[x]=mfma16h(T3[x-2],X3[0],Y[x]);
      if(x>=4) Y[x]=mfma16h(T3[x-4],X3[1],Y[x]);
    }
    const u32 ya=S_YC+(ch*2+sub)*6144+(64*th+rho)*4;
    #pragma unroll
    for(int x=0;x<6;x++){
      #pragma unroll
      for(int i=0;i<4;i++) *LDSP(float,ya+1024*x+64*i)=Y[x][i];
    }
  }
  if(w<4) prep_emit(b,S_PW,a.ws,w,lane);
  STAMP(0,9);
  LSYNC();
  { const int tbk=tid>>5, tt=(4*tid)&127;
    #pragma unroll
    for(int c=0;c<2;c++){
      const u32 ya=S_YD+c*2*YDB+(tid>>4)*272+(tid&15)*16;
      f32x4 y=*LDSP(f32x4,ya)+*LDSP(f32x4,ya+YDB);
      if(w>=2){ const u32 yc=S_YC+c*2*6144+(tid-128)*16; y+=*LDSP(f32x4,yc)+*LDSP(f32x4,yc+6144); }
      y*=invS[c];
      u32x2 hv;
      #pragma unroll
      for(int i=0;i<4;i+=2){
        u32 p=pkbf(y[i],y[i+1]);
        u32 q=pkbf(lo16(p)+ud[i][c],hi16(p)+ud[i+1][c]);
        hv[i>>1]=pkbf(xf[i][c]+lo16(q),xf[i+1][c]+hi16(q));
      }
      const int dd=d0+c;
      *(u32x2*)(a.ws+OFF_H+(size_t)(tbk*D+dd)*256+(((tt>>3)^hswz(dd))<<4)+(tt&7)*2)=hv;
    }
  }
  STAMP(0,10); STAMP_FLUSH(0);
}

constexpr int G_LDS=160*1024;
struct GArgs{ char* ws; u16* out; };

DI void stats_body(int b, char* ws){
  const int x=b&7, tb=(x>>1)*4+(b>>3), half=x&1, tid=threadIdx.x;
  const int c=tid&7, dg=tid>>3;
  u32x4 v[7];
  #pragma unroll
  for(int i=0;i<7;i++){ int d=dg+64*i; v[i]=*(const u32x4*)(ws+OFF_H+(size_t)(tb*D+d)*256+(((half*8+c)^hswz(d))<<4)); }
  f32x4 s0={0,0,0,0}, s1={0,0,0,0};
  #pragma unroll
  for(int i=0;i<7;i++){ f32x4 a={lo16(v[i][0]),hi16(v[i][0]),lo16(v[i][1]),hi16(v[i][1])}, bb={lo16(v[i][2]),hi16(v[i][2]),lo16(v[i][3]),hi16(v[i][3])}; s0+=a; s1+=bb; }
  float mean_t=0.f;
  #pragma unroll
  for(int pass=0;pass<2;pass++){
    *LDSP(f32x4,dg*272+c*32)=s0; *LDSP(f32x4,dg*272+c*32+16)=s1;
    LSYNC();
    { float t=0.f;
      #pragma unroll
      for(int k=0;k<8;k++) t+=*LDSP(float,((tid>>6)*8+k)*272+(tid&63)*4);
      *LDSP(float,20480+pass*2048+tid*4)=t; }
    LSYNC();
    if(pass==0){
      f32x4 m0={0,0,0,0}, m1={0,0,0,0};
      #pragma unroll
      for(int k=0;k<8;k++){ m0+=*LDSP(f32x4,20480+k*256+c*32); m1+=*LDSP(f32x4,20480+k*256+c*32+16); mean_t+=*LDSP(float,20480+k*256+(tid&63)*4); }
      m0*=(1.f/D); m1*=(1.f/D); mean_t*=(1.f/D);
      s0=f32x4{0,0,0,0}; s1=f32x4{0,0,0,0};
      #pragma unroll
      for(int i=0;i<7;i++){ f32x4 a={lo16(v[i][0]),hi16(v[i][0]),lo16(v[i][1]),hi16(v[i][1])}, bb={lo16(v[i][2]),hi16(v[i][2]),lo16(v[i][3]),hi16(v[i][3])}; a-=m0; bb-=m1; s0+=a*a; s1+=bb*bb; }
    }
  }
  if(tid<64){
    float q=0.f;
    #pragma unroll
    for(int k=0;k<8;k++) q+=*LDSP(float,20480+2048+k*256+tid*4);
    float sd=__builtin_amdgcn_sqrtf(rbf(rbf(q*(1.f/D))+1e-5f));
    u32 g=pkbf(mean_t,sd);
    __hip_atomic_store((GAS u32*)(ws+OFF_G2)+tb*128+half*64+tid,g,__ATOMIC_RELAXED,__HIP_MEMORY_SCOPE_AGENT);
  }
}

__global__ __launch_bounds__(512,2) void k_g(GArgs a){
  __shared__ __attribute__((aligned(16))) char smem[G_LDS];
  asm volatile("" :: "v"(smem));
  STAMP_DECL; STAMP(1,0);
  if(blockIdx.x<32){ stats_body(blockIdx.x,a.ws); STAMP(1,11); STAMP_FLUSH(1); return; }
  const int b=blockIdx.x-32, x=b&7, ii=b>>3;
  const int tb=(x>>1)*4+ii/7, half=x&1, jj=ii%7, j=7*half+jj;
  const int w=__builtin_amdgcn_readfirstlane(threadIdx.x>>6), lane=threadIdx.x&63;
  const int dsel=w&1, q4=w>>1;
  const u32 voff=lane*16;
  const u32x4 rsH=mkrs(a.ws+OFF_H+(size_t)tb*114688,114688);
  const u32x4 rsW=mkrs(a.ws+OFF_W1+(size_t)(j*8+w)*29*1024,29*1024);
  const u32x4 rsD=mkrs(a.ws+OFF_WD+(size_t)((2*jj+dsel)*112+q4*28)*1024,28*1024);
  const u32x4 rsA=mkrs(a.ws+OFF_ACT+(size_t)tb*4*112*1024,4*112*1024);
  const u32x4 rsS=mkrs(a.ws+OFF_G2+(size_t)tb*512,512);
  u32x4 wf[29], wd[28];
  u32 stg[4];
  f32x16 acc[4];
  u32x4 bq[2][4];
  u32 ba[4][2];
  const int g0=(jj+2*(ii/7))%7;
  u32 sg[7];
  #pragma unroll
  for(int c=0;c<7;c++){ int t=c+g0; sg[c]=(t>=7?t-7:t)*4096; }
  u32 hs[3];
  #pragma unroll
  for(int c=0;c<3;c++){ int t=2*c+(w>>2)+g0; hs[c]=(t>=7?t-7:t)*16384+(w&3)*4096; }
  const u32 hoA=w*4096, hoC=98304+w*2048, hsC=sg[6]*4+w*2048;
  asm volatile("s_nop 4");
  #define GLH(i) do{ if((i)<12) blds16<((i)&3)*1024>(voff,rsH,hs[((i)>>2)%3],hoA+((i)>>2)*32768); else blds16<((i)&1)*1024>(voff,rsH,hsC,hoC); }while(0)
  #define LDW(s) bld16<((s)&3)*1024>(wf[s],voff,rsW,(s)<28?sg[((s)>>2)%7]:28672u)
  #define LDD(s) bld16<((s)&3)*1024>(wd[s],voff,rsD,sg[(s)>>2])
  #define LDST(k) asm volatile("buffer_load_dword %0, %1, %2, 0 offen offset:%3 sc1" : "=v"(stg[k]) : "v"(svo), "s"(rsS), "i"((k)*128))
  #define G1_SETUP \
    const int g16=lane>>4, h=g16>>1, th=g16&1, q=(lane>>2)&3, pp=lane&3; \
    const u32 svo=(lane&31)*4; \
    _Pragma("unroll") for(int i=0;i<4;i++) _Pragma("unroll") for(int e=0;e<2;e++){ ba[i][e]=(8*h+4*e+q)*256+(((4*(i^q))|((2*th+(pp>>1))^(2*h+e)))<<4)+8*(pp&1); } \
    _Pragma("unroll") for(int i=0;i<4;i++) for(int r=0;r<16;r++) acc[i][r]=0.f
  #define RDF(dst,T,s) do{ u32x2 r0_=trread(ba[T][0]+(s)*4096), r1_=trread(ba[T][1]+(s)*4096); dst=u32x4{r0_[0],r0_[1],r1_[0],r1_[1]}; }while(0)
  #define RD4(s) do{ RDF(bq[(s)&1][0],0,s); RDF(bq[(s)&1][1],1,s); RDF(bq[(s)&1][2],2,s); RDF(bq[(s)&1][3],3,s); }while(0)
  #define MMT(T,s) acc[T]=mfma32(wf[s],bq[(s)&1][T],acc[T])
  #define RD1(T,s) do{ u32x2 r0_=trread(ba[T][0]+((s)-16)*4096), r1_=trread(ba[T][1]+((s)-16)*4096); bq[0][(s)&3]=u32x4{r0_[0],r0_[1],r1_[0],r1_[1]}; }while(0)
  #define REBASE do{ _Pragma("unroll") for(int i=0;i<4;i++) _Pragma("unroll") for(int e=0;e<2;e++){ ba[i][e]+=65536; asm("" : "+v"(ba[i][e])); } }while(0)
  #define MM1(T,s) acc[T]=mfma32(wf[s],bq[0][(s)&3],acc[T])
  #define MMC(T) do{ u32 nm_=(stg[T]&0xffffu)^0x8000u; nm_|=nm_<<16; u32x4 bx_={nm_,nm_,nm_,nm_}; acc[T]=mfma32(wf[28],bx_,acc[T]); }while(0)
  #define STATS_WAIT(NN) do{ u32 t_; u32 c_; \
    asm volatile("s_waitcnt vmcnt(%8)\n\ts_mov_b32 %5, 0x8000\n" \
      "1:\n\tv_min3_u32 %4, %0, %1, %2\n\tv_min_u32 %4, %4, %3\n\tv_cmp_eq_u32 vcc, 0, %4\n\ts_cbranch_vccz 2f\n\t" \
      "s_sub_u32 %5, %5, 1\n\ts_cbranch_scc1 2f\n\ts_sleep 1\n\t" \
      "buffer_load_dword %0, %6, %7, 0 offen sc1\n\tbuffer_load_dword %1, %6, %7, 0 offen offset:128 sc1\n\t" \
      "buffer_load_dword %2, %6, %7, 0 offen offset:256 sc1\n\tbuffer_load_dword %3, %6, %7, 0 offen offset:384 sc1\n\t" \
      "s_waitcnt vmcnt(0)\n\ts_branch 1b\n2:" \
      : "+v"(stg[0]),"+v"(stg[1]),"+v"(stg[2]),"+v"(stg[3]),"=&v"(t_),"=&s"(c_) : "v"(svo),"s"(rsS),"i"(NN) : "vcc","scc"); }while(0)
  const float c0=-2.f*0.7978845608028654f*1.4426950408889634f, c1=c0*0.044715f;
  float ek0,ek1,es2,ez0,ez1,eg0,eg1; u32x4 eo;
  #define OPQ(x) asm("" : "+v"(x))
  #define EPI(T,k) do{ constexpr int g_=(k)/3, st_=(k)%3; \
    if((k)==0){ float sd_=hi16(stg[T]); float ri_=__builtin_amdgcn_rcpf(sd_); ek0=c0*ri_; ek1=(c1*ri_)*(ri_*ri_); es2=sd_*sd_; } \
    if(st_==0){ float g0_=acc[T][2*g_], g1_=acc[T][2*g_+1]; ez0=g0_*__builtin_fmaf(g0_*g0_,ek1,ek0); OPQ(ez0); ez1=g1_*__builtin_fmaf(g1_*g1_,ek1,ek0); OPQ(ez1); } \
    else if(st_==1){ ez0=__builtin_fmaf(__builtin_amdgcn_exp2f(ez0),es2,es2); OPQ(ez0); ez1=__builtin_fmaf(__builtin_amdgcn_exp2f(ez1),es2,es2); OPQ(ez1); eg0=acc[T][2*g_]*acc[T][8+2*g_]; OPQ(eg0); } \
    else { eg1=acc[T][2*g_+1]*acc[T][9+2*g_]; OPQ(eg1); eg0*=__builtin_amdgcn_rcpf(ez0); OPQ(eg0); eg1*=__builtin_amdgcn_rcpf(ez1); OPQ(eg1); eo[g_]=pkbf(eg0,eg1); } }while(0)
  #define STACT(T) asm volatile("buffer_store_dwordx4 %0, %1, %2, %3 offen sc1\n\ts_nop 1" :: "v"(eo), "v"(voff), "s"(rsA), "s"((u32)(((T)*112+j*8+w)*1024)) : "memory")
  u32x4 hres;
  #define HRES do{ const int tau_=half*2+dsel; const int gs_=jj-g0; const u32 o_=((gs_<0?gs_+7:gs_)*4+q4)*4096-65536; \
    u32x2 r0_=trread((ba[0][0]^(tau_<<6))+o_), r1_=trread((ba[0][1]^(tau_<<6))+o_); hres=u32x4{r0_[0],r0_[1],r1_[0],r1_[1]}; }while(0)
  constexpr int T0=0,T1=1,T2=2,T3=3;
#include "g1_sched.inc"
  STAMP(1,4);
  waitv<0>(); LSYNC();
  STAMP(1,5);
  GAS u32* cnt=(GAS u32*)(a.ws+OFF_CNT+tb*256);
  if(threadIdx.x==0) __hip_atomic_fetch_add(cnt,1u,__ATOMIC_RELAXED,__HIP_MEMORY_SCOPE_AGENT);
  SB;
#include "gd_sched.inc"
  SB;
  if(w==0){
    u32 t_,c_;
    asm volatile("s_mov_b32 %1, 0x8000\n"
      "1:\n\tglobal_load_dword %0, %2, %3 sc1\n\ts_waitcnt vmcnt(0)\n\tv_cmp_gt_u32 vcc, 14, %0\n\ts_cbranch_vccz 2f\n\t"
      "s_sub_u32 %1, %1, 1\n\ts_cbranch_scc1 2f\n\ts_sleep 1\n\ts_branch 1b\n2:"
      : "=&v"(t_),"=&s"(c_) : "v"(0u),"s"(cnt) : "vcc","scc","memory");
  }
  BAR();
  STAMP(1,6);
  const u32 aso=((half*2+dsel)*112+q4*28)*1024, al=w*4096;
  #define GLA(c,k) blds16_sc1<(k)*1024>(voff,rsA,aso+sg[c],al+((c)%5)*32768)
  f32x16 o0,o1; for(int r=0;r<16;r++){ o0[r]=0.f; o1[r]=0.f; }
  u32 ar=q4*8192+voff, ar2=ar+65536, ar4=ar+131072; OPQ(ar2); OPQ(ar4);
  u32x4 rb[2][8];
  #define ARB(c) (((c)%5)<2?ar+((c)%5)*32768:(((c)%5)<4?ar2+(((c)%5)-2)*32768:ar4))
  #define RDA(c) do{ asm volatile("" ::: "memory"); _Pragma("unroll") for(int k=0;k<4;k++){ rb[(c)&1][2*k]=*LDSP(u32x4,ARB(c)+k*1024); rb[(c)&1][2*k+1]=*LDSP(u32x4,ARB(c)+4096+k*1024); } }while(0)
  #define G2SYNC(NN) do{ asm volatile("s_waitcnt vmcnt(%0) lgkmcnt(0)" :: "i"(NN) : "memory"); BAR(); }while(0)
  #define MMD(c,k,i) do{ if(i==0) o0=mfma32(wd[(c)*4+(k)],rb[(c)&1][2*(k)],o0); else o1=mfma32(wd[(c)*4+(k)],rb[(c)&1][2*(k)+1],o1); }while(0)
#include "g2_sched.inc"
  STAMP(1,8);
  constexpr int RS=272, RB=65536;
  { const int tok=lane&31, hh=lane>>5; const u32 ra=RB+(q4*64+tok)*RS+(dsel*32+4*hh)*4;
    #pragma unroll
    for(int g=0;g<4;g++){ *LDSP(f32x4,ra+32*g)=f32x4{o0[4*g],o0[4*g+1],o0[4*g+2],o0[4*g+3]}; *LDSP(f32x4,ra+32*RS+32*g)=f32x4{o1[4*g],o1[4*g+1],o1[4*g+2],o1[4*g+3]}; }
    LSYNC();
    STAMP(1,9);
    const int dl=q4*16+hh*8; const u32 rr=RB+(dsel*32+tok)*RS+dl*4;
    f32x4 s0=*LDSP(f32x4,rr), s1=*LDSP(f32x4,rr+16);
    #pragma unroll
    for(int qq=1;qq<4;qq++){ s0+=*LDSP(f32x4,rr+qq*64*RS); s1+=*LDSP(f32x4,rr+qq*64*RS+16); }
    u32 p0=pkbf(s0[0],s0[1]), p1=pkbf(s0[2],s0[3]), p2=pkbf(s1[0],s1[1]), p3=pkbf(s1[2],s1[3]);
    u32x4 ov={pkbf(lo16(hres[0])+lo16(p0),hi16(hres[0])+hi16(p0)),pkbf(lo16(hres[1])+lo16(p1),hi16(hres[1])+hi16(p1)),
              pkbf(lo16(hres[2])+lo16(p2),hi16(hres[2])+hi16(p2)),pkbf(lo16(hres[3])+lo16(p3),hi16(hres[3])+hi16(p3))};
    const int t=tb*128+(half*2+dsel)*32+tok;
    *(u32x4*)(a.out+(size_t)t*D+jj*64+dl)=ov;
  }
  STAMP(1,10); STAMP_FLUSH(1);
}

extern "C" void kernel_launch(void* const* d_in, const int* in_sizes, int n_in,
                              void* d_out, int out_size, void* d_ws, size_t ws_size,
                              hipStream_t stream) {
  SArgs sa;
  sa.x=(const u16*)d_in[0]; sa.Are=(const u16*)d_in[1]; sa.Aim=(const u16*)d_in[2]; sa.Bre=(const u16*)d_in[3]; sa.Bim=(const u16*)d_in[4];
  sa.Cre=(const u16*)d_in[5]; sa.Cim=(const u16*)d_in[6]; sa.logdt=(const u16*)d_in[7]; sa.Dskip=(const u16*)d_in[8];
  sa.wg=(const u16*)d_in[10]; sa.wu=(const u16*)d_in[11]; sa.wd=(const u16*)d_in[12]; sa.ws=(char*)d_ws;
  k_s<<<256,512,0,stream>>>(sa);
  GArgs ga; ga.ws=(char*)d_ws; ga.out=(u16*)d_out;
  k_g<<<256,512,0,stream>>>(ga);
}
```

```cpp
#include <hip/hip_runtime.h>
#include <stdint.h>
typedef unsigned short u16; typedef unsigned u32; typedef unsigned long long u64;
typedef __attribute__((ext_vector_type(4))) unsigned u32x4;
typedef __attribute__((ext_vector_type(2))) unsigned u32x2;
typedef __attribute__((ext_vector_type(4))) short s16x4;
typedef __attribute__((ext_vector_type(2))) __bf16 bf16x2;
typedef __attribute__((ext_vector_type(2))) _Float16 f16x2;
typedef __attribute__((ext_vector_type(8))) _Float16 f16x8;
typedef __attribute__((ext_vector_type(2))) float f32x2;
typedef __attribute__((ext_vector_type(4))) float f32x4;
typedef __attribute__((ext_vector_type(16))) float f32x16;
#define DI __device__ __forceinline__
#define LDSP(T, off) ((__attribute__((address_space(3))) T*)(uintptr_t)(u32)(off))
#define GAS __attribute__((address_space(1)))
constexpr int L=2048, D=448, N=256, F=1792;

DI float bf2f(u16 v){ return __uint_as_float(((unsigned)v)<<16); }
DI float lo16(u32 v){ return __uint_as_float(v<<16); }
DI float hi16(u32 v){ return __uint_as_float(v&0xffff0000u); }
DI u32 pkbf(float a,float b){ f32x2 v={a,b}; return __builtin_bit_cast(u32,__builtin_convertvector(v,bf16x2)); }
DI u32 pkbf2(f32x2 v){ return __builtin_bit_cast(u32,__builtin_convertvector(v,bf16x2)); }
DI float rbf(float f){ return lo16(pkbf(f,f)); }
DI u32 pkh(float a,float b){ f32x2 v={a,b}; return __builtin_bit_cast(u32,__builtin_convertvector(v,f16x2)); }
DI u32 pkh2(f32x2 v){ return __builtin_bit_cast(u32,__builtin_convertvector(v,f16x2)); }
DI f32x16 mfma32h(u32x4 a,u32x4 b,f32x16 c){ return __builtin_amdgcn_mfma_f32_32x32x16_f16(__builtin_bit_cast(f16x8,a),__builtin_bit_cast(f16x8,b),c,0,0,0); }
DI f32x4 mfma16h(u32x4 a,u32x4 b,f32x4 c){ return __builtin_amdgcn_mfma_f32_16x16x32_f16(__builtin_bit_cast(f16x8,a),__builtin_bit_cast(f16x8,b),c,0,0,0); }
DI u32x2 trread(u32 addr){ return __builtin_bit_cast(u32x2,__builtin_amdgcn_ds_read_tr16_b64_v4i16(LDSP(s16x4,addr))); }
DI u32x4 mkrs(const void* p,u32 bytes){ u64 a=(u64)p; u32x4 r={(u32)a,(u32)(a>>32)&0xffffu,bytes,0x00020000u}; return r; }
template<int OFF> DI void blds16(u32 voff,u32x4 rs,u32 soff,u32 lds_dst){
  asm volatile("s_mov_b32 m0, %3\n\ts_nop 0\n\tbuffer_load_dwordx4 %0, %1, %2 offen offset:%4 lds" :: "v"(voff), "s"(rs), "s"(soff), "s"(lds_dst), "i"(OFF) : "memory");
}
template<int OFF> DI void blds16_sc1(u32 voff,u32x4 rs,u32 soff,u32 lds_dst){
  asm volatile("s_mov_b32 m0, %3\n\ts_nop 0\n\tbuffer_load_dwordx4 %0, %1, %2 offen offset:%4 sc1 lds" :: "v"(voff), "s"(rs), "s"(soff), "s"(lds_dst), "i"(OFF) : "memory");
}
template<int OFF> DI void bldsq(u32 voff,u32x4 rs,u32 soff){
  asm volatile("buffer_load_dwordx4 %0, %1, %2 offen offset:%3 lds" :: "v"(voff), "s"(rs), "s"(soff), "i"(OFF) : "memory");
}
DI void bldsq_sc1(u32 voff,u32x4 rs,u32 soff){
  asm volatile("buffer_load_dwordx4 %0, %1, %2 offen sc1 lds" :: "v"(voff), "s"(rs), "s"(soff) : "memory");
}
template<int ADD> DI void m0_add(u32 base){ asm volatile("s_add_u32 m0, %0, %1" :: "s"(base), "i"(ADD) : "scc"); }
template<int OFF> DI void bld16(u32x4& dst,u32 voff,u32x4 rs,u32 soff){
  asm volatile("buffer_load_dwordx4 %0, %1, %2, %3 offen offset:%4 sc1" : "=v"(dst) : "v"(voff), "s"(rs), "s"(soff), "i"(OFF));
}
template<int NN> DI void waitv(){ asm volatile("s_waitcnt vmcnt(%0)" :: "i"(NN) : "memory"); }
template<int NN> DI void waitw(u32x4& r){ asm volatile("s_waitcnt vmcnt(%1)" : "+v"(r) : "i"(NN)); }
#define WAITL0() asm volatile("s_waitcnt lgkmcnt(0)" ::: "memory")
#define BAR() do{ __builtin_amdgcn_s_barrier(); asm volatile("" ::: "memory"); }while(0)
#define SB __builtin_amdgcn_sched_barrier(0)
#define LSYNC() do{ WAITL0(); BAR(); }while(0)

constexpr size_t OFF_H   = 0;
constexpr size_t OFF_ACT = 8u<<20;
constexpr size_t OFF_GR  = 16u<<20;
constexpr size_t OFF_EP  = OFF_GR+16384;
constexpr size_t OFF_G2  = OFF_GR+32768;
constexpr size_t OFF_FL  = OFF_G2+16384;
constexpr int FL_H=0;
constexpr int FL_C=32*128;
constexpr int FL_SZ=64*128;
constexpr size_t OFF_XG  = 17u<<20;
constexpr int HT=D*64;

typedef f32x2 cf;
DI cf cmul(cf x,cf w){ cf t,r;
  asm("v_pk_mul_f32 %0, %1, %2 op_sel_hi:[0,1]" : "=v"(t) : "v"(x), "v"(w));
  asm("v_pk_fma_f32 %0, %1, %2, %3 op_sel:[1,1,0] op_sel_hi:[1,0,1] neg_lo:[0,1,0]" : "=v"(r) : "v"(x), "v"(w), "v"(t));
  return r; }
DI void cmul2(cf& r0,cf& r1,cf x0,cf x1,cf w){ cf t0,t1;
  asm("v_pk_mul_f32 %2, %4, %6 op_sel_hi:[0,1]\n\tv_pk_mul_f32 %3, %5, %6 op_sel_hi:[0,1]\n\t"
      "v_pk_fma_f32 %0, %4, %6, %2 op_sel:[1,1,0] op_sel_hi:[1,0,1] neg_lo:[0,1,0]\n\tv_pk_fma_f32 %1, %5, %6, %3 op_sel:[1,1,0] op_sel_hi:[1,0,1] neg_lo:[0,1,0]"
      : "=&v"(r0), "=&v"(r1), "=&v"(t0), "=&v"(t1) : "v"(x0), "v"(x1), "v"(w)); }
DI cf cexpz(float a2,float br,float l){ float e=__builtin_amdgcn_exp2f(a2*l); float p=__builtin_amdgcn_fractf(br*l); cf r={__builtin_amdgcn_cosf(p),__builtin_amdgcn_sinf(p)}; return r*e; }

constexpr int TS=2064;
constexpr int S_LDS = 160*1024;
constexpr int S_U   = S_LDS-24704;
constexpr int UPAD=576, UB=42*144;
constexpr int S_KR  = S_U+2*UB;
constexpr int KRB=1216;
constexpr int S_CT  = S_KR+4*KRB;
constexpr int CTB=192;
constexpr int S_SP  = S_CT+24*CTB;
constexpr int S_EX  = S_U+24576;
constexpr int XS=912;
constexpr int S_XB  = S_U-8*XS;
constexpr int YDB=32*272;
constexpr int S_YD=0, S_YC=4*YDB;
constexpr int S_W1  = S_YC+4*6144;
static_assert((S_U&15)==0 && (S_XB&15)==0 && S_SP+256<=S_EX && 53*TS<=S_XB && S_W1+9*8192<=S_U && (S_W1&15)==0,"lds");
DI constexpr int uaddr(int s){ return 2*(s+UPAD)+16*((s+UPAD)>>6); }
template<int OFF> DI void put_tid(u32 v){ asm volatile("ds_write_addtid_b32 %0 offset:%1" :: "v"(v), "i"(OFF) : "memory"); }
template<int OFF> DI void put_tidf(float v){ asm volatile("ds_write_addtid_b32 %0 offset:%1" :: "v"(v), "i"(OFF) : "memory"); }
DI void set_m0(u32 v){ asm volatile("s_mov_b32 m0, %0\n\ts_nop 0" :: "s"(v)); }
#define AST(T,p,v) __hip_atomic_store((GAS T*)(p),(T)(v),__ATOMIC_RELAXED,__HIP_MEMORY_SCOPE_AGENT)
#define AADD(p) __hip_atomic_fetch_add((GAS u32*)(p),1u,__ATOMIC_RELAXED,__HIP_MEMORY_SCOPE_AGENT)
#define ALD(T,p) __hip_atomic_load((GAS T*)(p),__ATOMIC_RELAXED,__HIP_MEMORY_SCOPE_AGENT)

DI void stats_body(int t2, char* ws){
  const int tid=threadIdx.x, c=tid&7, dg=tid>>3;
  auto rsH=__builtin_amdgcn_make_buffer_rsrc((void*)(ws+OFF_H),0,L*D*2,0x00020000);
  u32x4 v[7];
  #pragma unroll
  for(int i=0;i<7;i++) v[i]=__builtin_amdgcn_raw_buffer_load_b128(rsH,(c>>2)*HT+dg*64+(c&3)*16+i*4096,t2*2*HT,16);
  const u32x4 pv=__builtin_amdgcn_raw_buffer_load_b128(rsH,(c>>2)*HT+(c&3)*16,t2*2*HT,16);
  const f32x4 p0={lo16(pv[0]),hi16(pv[0]),lo16(pv[1]),hi16(pv[1])}, p1={lo16(pv[2]),hi16(pv[2]),lo16(pv[3]),hi16(pv[3])};
  f32x4 s0={0,0,0,0}, s1={0,0,0,0}, q0={0,0,0,0}, q1={0,0,0,0};
  #pragma unroll
  for(int i=0;i<7;i++){ f32x4 a={lo16(v[i][0]),hi16(v[i][0]),lo16(v[i][1]),hi16(v[i][1])}, bb={lo16(v[i][2]),hi16(v[i][2]),lo16(v[i][3]),hi16(v[i][3])};
    a-=p0; bb-=p1; s0+=a; s1+=bb; q0+=a*a; q1+=bb*bb; }
  *LDSP(f32x4,dg*528+c*32)=s0; *LDSP(f32x4,dg*528+c*32+16)=s1; *LDSP(f32x4,dg*528+256+c*32)=q0; *LDSP(f32x4,dg*528+256+c*32+16)=q1;
  if(dg==0){ *LDSP(f32x4,36864+2048+c*32)=p0; *LDSP(f32x4,36864+2048+c*32+16)=p1; }
  LSYNC();
  { float t=0.f;
    #pragma unroll
    for(int k=0;k<16;k++) t+=*LDSP(float,((tid>>7)*16+k)*528+(tid&127)*4);
    *LDSP(float,36864+tid*4)=t; }
  LSYNC();
  if(tid<64){
    float s=0.f,q=0.f;
    #pragma unroll
    for(int k=0;k<4;k++){ s+=*LDSP(float,36864+k*512+tid*4); q+=*LDSP(float,36864+k*512+256+tid*4); }
    s*=(1.f/D); q=fmaxf(q*(1.f/D)-s*s,0.f);
    float sd=__builtin_amdgcn_sqrtf(rbf(rbf(q)+1e-5f));
    AST(u32,ws+OFF_G2+(t2*64+tid)*4,pkbf(s+*LDSP(float,36864+2048+tid*4),sd));
  }
}

struct SArgs{ const u16 *x,*Are,*Aim,*Bre,*Bim,*Cre,*Cim,*logdt,*Dskip,*wg,*wu,*wd; char* ws; u16* out; };

__global__ __launch_bounds__(512,2) void k_all(SArgs a){
  __shared__ __attribute__((aligned(16))) char smem[S_LDS];
  u32x4 cpf;
  {
    const u64 pc=__builtin_amdgcn_s_getpc(); u32 o=((blockIdx.x>>3)&3)*8192+threadIdx.x*16; o=o<31744?o:0;
    asm volatile("global_load_dwordx4 %0, %1, %2" : "=v"(cpf) : "v"(o), "s"(pc&~63ull));
    SB;
  }
  asm volatile("" :: "s"((u32)(uintptr_t)(__attribute__((address_space(3))) char*)smem));
  const int b=blockIdx.x, tid=threadIdx.x;
  const int w=__builtin_amdgcn_readfirstlane(tid>>6), lane=tid&63;
  auto rsX=__builtin_amdgcn_make_buffer_rsrc((void*)a.x,0,L*D*2,0x00020000);
  auto rsG=__builtin_amdgcn_make_buffer_rsrc((void*)(a.ws+OFF_GR),0,L*8,0x00020000);
  auto rsT=__builtin_amdgcn_make_buffer_rsrc((void*)(a.ws+OFF_XG),0,224*L*8,0x00020000);
  char* const fl=a.ws+OFF_FL;
  const int row=b*8+w;
  u32x4 xr=__builtin_amdgcn_raw_buffer_load_b128(rsX,lane<56?lane*16:0x7ffffff0,row*(D*2),0);
  const u32 epoch=*(const __attribute__((address_space(4))) u32*)(a.ws+OFF_EP);
  const bool isS=b<224;
  const int pr=isS?((b&~15)|((b&7)<<1)|((b>>3)&1)):(b-224)*7, d0=2*pr;
  const int ch=w>>2, n=tid&255;
  const u32 po=(u32)(d0*N+tid)*2u;
  #define LDP(p) (*(const u16*)((const char*)(p)+po))
  const u32 pT=*(const __attribute__((address_space(4))) u32*)(a.logdt+d0);
  const u16 pAr=LDP(a.Are), pAi=LDP(a.Aim), pBr=LDP(a.Bre), pBi=LDP(a.Bim), pCr=LDP(a.Cre), pCi=LDP(a.Cim);
  const u32 pD=*(const __attribute__((address_space(4))) u32*)(a.Dskip+d0);
  const int hp=b-224, ht2=(((hp&7)>>1)*4+(hp>>3))*2+(hp&1);
  if(!isS&&w==0){
    AST(u32,a.ws+OFF_G2+(ht2*64+lane)*4,0u);
    if(lane<2) AST(u32,fl+(hp*2+lane)*128,0u);
  }
  SB;
  #define PIN(x) asm volatile("" : "+v"(x))
  #define PINS(x) asm volatile("" : "+s"(x))
  { u32x4 z={0,0,0,0};
    #pragma unroll
    for(int i=0;i<3;i++) *LDSP(u32x4,S_U+tid*16+i*8192)=z; }
  const bool isD=w>=4;
  const int sub=w&1, cch=(w>>1)&1;
  u32 ca0,ca1,ca1b,ca2,cs0,cs1,cra;
  { const int kr=S_KR+ch*2*KRB, ct=S_CT+ch*12*CTB, r=n>>5, j=n&31, dump=S_XB+16;
    ca0=kr+2*(544-(r+8*j));
    if((w&2)==0){ ca1=kr+2*(544-(256+2*r+8*j)); cs0=(r&1)?dump:ca1-2; cs1=((r&1)||n==0)?dump:ca1+2; }
    else { int i2=64*((r-4)>>1)+(r&1)+2*j; int ph=(i2-1)&3, q2=(i2-1)>>2;
      ca1=i2==0?S_SP+ch*4:ct+((ph>>1)*3+(ph&1))*2*CTB+2*(64-q2); cs0=i2==0?kr+2*(544-511):dump; cs1=dump; }
    { int ps=(n-1)&1, q3=(n-1)>>1; ca2=n==0?S_SP+ch*4+2:(n>128?dump:ct+(ps*3+2)*2*CTB+2*(64-q3)); }
    cra=ch*1024+(r&3)*TS+(r>>2)*128+j*4;
    ca1b=ca1+((w&2)?CTB+2:KRB+2); PIN(ca1b); PIN(ca0); PIN(ca1); PIN(ca2); PIN(cs0); PIN(cs1); PIN(cra);
  }
  const int x=b&7, ii=b>>3;
  const int tb=(x>>1)*4+ii/7, half=x&1, jj=ii%7, j=7*half+jj;
  const int gr0=(jj+2*(ii/7))%7;
  u32 sg[7];
  #pragma unroll
  for(int c=0;c<7;c++){ int t=c+gr0; sg[c]=(t>=7?t-7:t); PINS(sg[c]); }
  u32x4 rsW=mkrs((const char*)(w<4?a.wg:a.wu)+j*256,D*F*2-j*256);
  u32 wvo=(4*(w&3)+(lane>>4))*(F*2)+(((lane&15)^(2*((lane>>4)|(w&4))))<<4);
  u32 xwa=S_XB+w*XS+lane*16, xra=S_XB+2*(tid&3)*XS+(tid>>2)*4, xso_=(tid>>3)*32768+(tid&7)*16+b*128;
  u32 gvo=tid*32, xvo=(tid>>1)*128+(tid&1)*32+(pr>>1)*32768+(pr&1)*64;
  u32 fa=w*256+(lane&31)*TS+(lane>>5)*16;
  u32 ua=S_U+uaddr(4*tid), eya=S_YD+(tid>>4)*272+(tid&15)*16, eyc=w>=2?S_YC+(tid-128)*16:S_U+lane*16, hso=(tid>>3)*HT+(tid&1)*64+((tid>>1)&3)*16+d0*64;
  PINS(rsW); PIN(wvo); PIN(xwa); PIN(xra); PIN(xso_); PIN(gvo); PIN(xvo); PIN(fa); PIN(ua); PIN(eya); PIN(eyc); PIN(hso);
  SB;
  if(lane<56) *LDSP(u32x4,xwa)=xr;
  u32 tag=__builtin_amdgcn_readfirstlane(epoch)+1u;
  float a2,br,eg,e0; cf cb; u32 ex;
  { const float pv=lo16(__builtin_amdgcn_readfirstlane(xr[0])), pl_=lane<56?pv:0.f;
    const f32x2 p2={pl_,pl_}; f32x2 v[4];
    #pragma unroll
    for(int i=0;i<4;i++){ v[i]=f32x2{lo16(xr[i]),hi16(xr[i])}-p2; }
    f32x2 s2=(v[0]+v[1])+(v[2]+v[3]), q2=__builtin_elementwise_fma(v[0],v[0],v[1]*v[1])+__builtin_elementwise_fma(v[2],v[2],v[3]*v[3]);
    float rs=s2[0]+s2[1], rq=q2[0]+q2[1];
    float ld=ch?hi16(pT):lo16(pT); float ph=ld*1.4426950408889634f; float pl=__builtin_fmaf(ld,1.4426950408889634f,-ph)+ld*1.9259629911e-8f;
    float e1=__builtin_amdgcn_exp2f(ph); float dt=rbf(__builtin_fmaf(e1*pl,0.6931471805599453f,e1));
    a2=(dt*bf2f(pAr))*1.4426950408889634f; br=(dt*bf2f(pAi))*0.15915494309189535f;
    cf B={dt*bf2f(pBr),dt*bf2f(pBi)}, C={bf2f(pCr),bf2f(pCi)};
    cb=cmul(C,B);
    float g=fmaxf(a2,0.f);
    eg=__builtin_amdgcn_exp2f(248.f*g); e0=__builtin_amdgcn_exp2f(-248.f*g);
    float mag=(fabsf(cb[0])+fabsf(cb[1]))*__builtin_amdgcn_exp2f(2296.f*g);
    #define R3(C) "v_add_f32_dpp %0, %0, %0 " C "\n\tv_add_f32_dpp %1, %1, %1 " C "\n\tv_max_u32_dpp %2, %2, %2 " C "\n\t"
    asm volatile("s_nop 1\n\t" R3("row_shr:1 row_mask:0xf bank_mask:0xf bound_ctrl:1") R3("row_shr:2 row_mask:0xf bank_mask:0xf bound_ctrl:1")
      R3("row_shr:4 row_mask:0xf bank_mask:0xf bound_ctrl:1") R3("row_shr:8 row_mask:0xf bank_mask:0xf bound_ctrl:1")
      R3("row_bcast:15 row_mask:0xa bank_mask:0xf") R3("row_bcast:31 row_mask:0xc bank_mask:0xf") "s_nop 0" : "+v"(rs), "+v"(rq), "+v"(mag));
    float ms=rs*(1.f/D), var=fmaxf(__builtin_fmaf(-ms,ms,rq*(1.f/D)),0.f);
    float sd=__builtin_amdgcn_sqrtf(rbf(rbf(var)+1e-5f));
    ex=(u32)__builtin_amdgcn_readlane(__builtin_bit_cast(int,mag),63)>>23; ex=ex<6u?6u:(ex>254u?254u:ex);
    if(lane==63){ *LDSP(u32,S_XB+w*XS+896)=pkbf(pv+ms,sd); *LDSP(u32,S_EX+w*4)=ex; }
    asm volatile("" :: "v"(cpf), "v"(sd));
    asm volatile("" : "+s"(tag));
  }
  waitv<0>(); LSYNC();
  { u32x4 v={*LDSP(u32,xra),tag,*LDSP(u32,xra+XS),tag};
    __builtin_amdgcn_raw_buffer_store_b128(v,rsT,xso_,0,16);
    if(w<6){ u32x4 v2={*LDSP(u32,xra+512),tag,*LDSP(u32,xra+512+XS),tag}; __builtin_amdgcn_raw_buffer_store_b128(v2,rsT,xso_+64*32768,0,16); }
    else if(tid<388){ u32x4 v2={*LDSP(u32,xra+512),tag,*LDSP(u32,xra+512+XS),tag}; __builtin_amdgcn_raw_buffer_store_b128(v2,rsG,(tid&3)*16+b*64,0,16); }
  }
  if(!isS){
    if(w==0){
      int spins=0;
      while(true){ u64 g=ALD(u64,a.ws+OFF_GR+(224+(lane&31))*64); if(__all((u32)(g>>32)==tag)||++spins>(1<<15)) break; __builtin_amdgcn_s_sleep(4); }
      while(true){ if(ALD(u32,a.ws+OFF_EP)==tag||++spins>(1<<15)) break; __builtin_amdgcn_s_sleep(4); }
      __builtin_amdgcn_s_sleep(20);
      *(volatile __attribute__((address_space(3))) u32*)LDSP(u32,S_U)=1u;
      while(true){ u32 v=ALD(u32,fl+FL_H+(lane&31)*128);
        if(__all(v==7u)||++spins>(1<<15)) break; __builtin_amdgcn_s_sleep(2); }
    } else {
      u32 t0=0,t1=0;
      #pragma unroll 1
      for(int it=0;it<3+7;it++){
        const int idx=((hp>>3)*7+w-1)*64+lane;
        const char* p;
        if(it<3){ int r1=it+((b&7)>>1)*2; r1=r1>=7?r1-7:r1; const int ln=(r1*1792+idx)>>1, r=ln/14, c=ln-r*14; p=(const char*)((idx&1)?a.wu:a.wg)+r*(F*2)+(b&1)*F+c*128; }
        else { int r2=it-3+(b&7); r2=r2>=7?r2-7:r2; r2=r2>=7?r2-7:r2; p=(const char*)a.wd+(r2*1792+idx)*128; }
        asm volatile("s_waitcnt vmcnt(0)\n\tglobal_load_dword %1, %2, off" : "+v"(t0), "+v"(t1) : "v"(p) : "memory");
        if(*(volatile __attribute__((address_space(3))) u32*)LDSP(u32,S_U)!=0u) break;
      }
      asm volatile("s_waitcnt vmcnt(0)" : "+v"(t0), "+v"(t1) :: "memory");
    }
    BAR();
    stats_body(ht2,a.ws);
    return;
  }
  u32x4 g0,g1,q0,q1;
  #define POLL() do{ g0=__builtin_amdgcn_raw_buffer_load_b128(rsG,gvo,0,16); g1=__builtin_amdgcn_raw_buffer_load_b128(rsG,gvo+16,0,16); \
    q0=__builtin_amdgcn_raw_buffer_load_b128(rsT,xvo,0,16); q1=__builtin_amdgcn_raw_buffer_load_b128(rsT,xvo+16,0,16); }while(0)
  #define PRIO(n) __builtin_amdgcn_s_setprio(n)
  PRIO(3);
  f32x2 invS;
  { u32x4 e0_=*LDSP(u32x4,S_EX), e1_=*LDSP(u32x4,S_EX+16);
    const u32 m0=__builtin_amdgcn_readfirstlane(max(max(e0_[0],e0_[1]),max(e0_[2],e0_[3]))), m1_=__builtin_amdgcn_readfirstlane(max(max(e1_[0],e1_[1]),max(e1_[2],e1_[3])));
    invS=f32x2{__uint_as_float((m0-5u)<<23),__uint_as_float((m1_-5u)<<23)};
    cb*=eg*__uint_as_float((259u-(ch?m1_:m0))<<23);
    cf v1=cexpz(a2,br,1.f), w8=cexpz(a2,-br,8.f);
    cf G=cexpz(a2,br,256.f);
    #define PUT(r,val) put_tid<(r)*TS>(pkh2(val))
    set_m0(w*256+21*TS);
    { cf w16=cmul(w8,w8); cf ea={e0,0.f}; cf eb=w8*e0;
      #define ESTEP(j) { cf na_=ea,nb_=eb; if((j)<15) cmul2(na_,nb_,ea,eb,w16); PUT(2*(j),ea); PUT(2*(j)+1,eb); ea=na_; eb=nb_; }
      ESTEP(0) ESTEP(1) ESTEP(2) ESTEP(3) PRIO(2); ESTEP(4) ESTEP(5) ESTEP(6) ESTEP(7) ESTEP(8) ESTEP(9) ESTEP(10) ESTEP(11) PRIO(1); ESTEP(12) ESTEP(13) ESTEP(14) ESTEP(15) }
    u32x4 Ef[8];
    #pragma unroll
    for(int s=0;s<8;s++) Ef[s]=*LDSP(u32x4,fa+21*TS+32*s);
    set_m0(w*256);
    { cf m1=v1, m2=cmul(v1,v1), m4=cmul(m2,m2);
      cf x0=cb, x1=cmul(cb,m1), x2,x3,x4,x5,x6,x7,G2,y1,z1,t0,t1,y2,G4,y3,y4;
      cmul2(x2,x3,x0,x1,m2); cmul2(x4,x5,x0,x1,m4); cmul2(G2,y1,G,x0,G);
      PUT(0,x0); PUT(1,x1);
      cmul2(x6,x7,x2,x3,m4); PUT(2,x2); PUT(3,x3);
      cmul2(z1,t0,x4,x2,G); PUT(4,x4); PUT(5,x5);
      cmul2(G4,y2,G2,x0,G2); PUT(6,x6); PUT(7,x7);
      PRIO(0);
      t1=cmul(x6,G); PUT(8,y1); PUT(9,t0); PUT(10,z1);
      cmul2(y3,t0,y1,x4,G2); PUT(11,t1); PUT(12,y2);
      cmul2(y4,t1,x0,y1,G4); PUT(13,t0); PUT(14,y3);
      z1=cmul(z1,G2); PUT(16,y4); PUT(17,t1);
      cmul2(t0,t1,y2,y3,G4); PUT(15,z1);
      y4=cmul(y4,G4); PUT(18,t0); PUT(19,t1); PUT(20,y4); }
    PRIO(3);
    POLL();
    u32x4 Af[8];
    #pragma unroll
    for(int s=0;s<8;s++) Af[s]=*LDSP(u32x4,fa+32*s);
    SB;
    f32x16 acc; for(int r=0;r<16;r++) acc[r]=0.f;
    #pragma unroll
    for(int s=0;s<8;s++) acc=mfma32h(Af[s],Ef[s],acc);
    asm volatile("s_nop 7\n\ts_nop 3" : "+v"(acc));
    put_tidf<0*TS>(acc[0]); put_tidf<1*TS>(acc[1]); put_tidf<2*TS>(acc[2]); put_tidf<3*TS>(acc[3]); put_tidf<4*TS>(acc[4]); put_tidf<5*TS>(acc[5]);
    put_tidf<6*TS>(acc[6]); put_tidf<7*TS>(acc[7]); put_tidf<8*TS>(acc[8]); put_tidf<9*TS>(acc[9]); put_tidf<10*TS>(acc[10]); put_tidf<11*TS>(acc[11]);
  }
  PRIO(0);
  u32x4 band[4]; u32 cva,cvb,cvc,cvy;
  if(isD){
    cva=S_KR+cch*2*KRB+(lane&1)*(KRB+2)+2*(32-(lane&31)+8*(lane>>5)+16*sub);
    cvb=S_U+cch*UB+144*(lane&31)+16*(lane>>5)+32*sub; cvc=cva+1024;
    cvy=S_YD+(cch*2+sub)*YDB+(lane&31)*272+(lane>>5)*16;
    asm volatile("" : "=v"(band[0]), "=v"(band[1]), "=v"(band[2]), "=v"(band[3]));
  } else {
    const int rho=lane&15, th=lane>>4, c0_=rho-8*th;
    const int lo[4]={11,6,sub?4:5,sub?4:14}, wid[4]={3,5,sub?9:7,sub?7:3};
    #pragma unroll
    for(int ty=0;ty<4;ty++){
      const int t=min(max(12-lo[ty]-c0_,0),31);
      u32 m=((((1u<<wid[ty])-1u)<<12)>>t)&0xffu; m|=m<<15;
      #pragma unroll
      for(int e2=0;e2<4;e2++) band[ty][e2]= ty<3 ? (m&(0x10001u<<(2*e2)))*(0x3c00u>>(2*e2)) : ((m>>(2*e2))&0x10001u);
    }
    cva=S_CT+cch*12*CTB+sub*6*CTB+(rho&1)*(CTB+2)+2*(64-rho+4*th)-96;
    cvb=S_U+cch*UB+uaddr(16*rho+8*th+(sub?-24:-16));
    cvc=S_U+cch*UB+uaddr(16*rho+8*th+(sub?-520:-16));
    cvy=S_YC+(cch*2+sub)*6144+(64*th+rho)*4;
  }
  PIN(band[0]); PIN(band[1]); PIN(band[2]); PIN(band[3]); PIN(cva); PIN(cvb); PIN(cvc); PIN(cvy);
  LSYNC();
  float pk_[3][4];
  #pragma unroll
  for(int it=0;it<3;it++){
    #pragma unroll
    for(int q=0;q<4;q++) pk_[it][q]=*LDSP(float,cra+it*4*TS+q*256); }
  asm volatile("" ::: "memory"); SB;
  u32 xg[4], un[4];
  {
    #define BADTAG() __any((((g0[1]^tag)|(g0[3]^tag)|(g1[1]^tag))|((g1[3]^tag)|(q0[1]^tag)|(q0[3]^tag))|((q1[1]^tag)|(q1[3]^tag)))!=0u)
    if(__builtin_expect(BADTAG(),0)){ int spins=0;
      do{ __builtin_amdgcn_s_sleep(2); asm volatile("" ::: "memory"); POLL(); }while(BADTAG()&&++spins<(1<<16)); }
  { float val[3];
    #pragma unroll
    for(int it=0;it<3;it++) val[it]=(pk_[it][0]+pk_[it][1])+(pk_[it][2]+pk_[it][3]);
    _Float16 h0=(_Float16)val[0], h1=(_Float16)val[1], h2=(_Float16)val[2];
    *LDSP(_Float16,ca0)=h0; *LDSP(_Float16,ca0+KRB+2)=h0;
    *LDSP(_Float16,ca2)=h2; *LDSP(_Float16,ca2+CTB+2)=h2;
    *LDSP(_Float16,ca1)=h1; *LDSP(_Float16,cs0)=h1; *LDSP(_Float16,cs0+KRB+2)=h1;
    *LDSP(_Float16,ca1b)=h1; *LDSP(_Float16,cs1)=h1; *LDSP(_Float16,cs1+KRB+2)=h1;
  }
    const u32 st[4]={g0[0],g0[2],g1[0],g1[2]};
    xg[0]=q0[0]; xg[1]=q0[2]; xg[2]=q1[0]; xg[3]=q1[2];
    float ri[4]; f32x2 t2[4]; u32 c[4];
    #pragma unroll
    for(int i=0;i<4;i++){ ri[i]=__builtin_amdgcn_rcpf(hi16(st[i])); const float m=lo16(st[i]); t2[i]=f32x2{lo16(xg[i]),hi16(xg[i])}-f32x2{m,m}; }
    SB;
    #pragma unroll
    for(int i=0;i<4;i++) c[i]=pkbf2(t2[i]);
    SB;
    #pragma unroll
    for(int i=0;i<4;i++) t2[i]=f32x2{lo16(c[i]),hi16(c[i])};
    SB;
    #pragma unroll
    for(int i=0;i<4;i++) t2[i]*=f32x2{ri[i],ri[i]};
    SB;
    #pragma unroll
    for(int i=0;i<4;i++) un[i]=pkbf2(t2[i]);
    SB;
  }
  float uu[2][4];
  #pragma unroll
  for(int i=0;i<4;i++){ uu[0][i]=lo16(un[i]); uu[1][i]=hi16(un[i]); }
  #pragma unroll
  for(int c=0;c<2;c++){ u32x2 v={pkh(uu[c][0],uu[c][1]),pkh(uu[c][2],uu[c][3])}; *LDSP(u32x2,ua+c*UB)=v; }
  LSYNC();
  if(b==0&&tid==0) AST(u32,a.ws+OFF_EP,tag);
  #define WSLOT(s) ((s)<9?S_W1+(s)*8192:((s)<22?2*HT+((s)-9)*8192:4*HT+((s)-22)*8192))
  #define WST(s) blds16<0>(wvo,rsW,(sg[(s)>>2]*4+((s)&3))*(16*F*2),WSLOT(s)+w*1024)
  f32x2 xt[2][2], ut[2][2];
  #define UTX(c,i) do{ const float Dc=(c)?hi16(pD):lo16(pD); float p0_=uu[c][2*(i)]*Dc, p1_=uu[c][2*(i)+1]*Dc; asm volatile("" : "+v"(p0_), "+v"(p1_)); \
    u32 q_=pkbf(p0_,p1_); ut[c][i]=f32x2{lo16(q_),hi16(q_)}; xt[c][i]=(c)?f32x2{hi16(xg[2*(i)]),hi16(xg[2*(i)+1])}:f32x2{lo16(xg[2*(i)]),lo16(xg[2*(i)+1])}; \
    asm volatile("" : "+v"(ut[c][i]), "+v"(xt[c][i])); }while(0)
  if(isD){
    f32x16 a0,a1; u32x4 A[17], B[18];
    #define RA(i) (A[i]=u32x4{*LDSP(u32,((i)<16?cva+64*(i):cvc)),*LDSP(u32,((i)<16?cva+64*(i):cvc)+4),*LDSP(u32,((i)<16?cva+64*(i):cvc)+8),*LDSP(u32,((i)<16?cva+64*(i):cvc)+12)})
    #define RB(i) (B[i]=*LDSP(u32x4,cvb+uaddr(-512+32*(i))))
    #define MD(acc_,i,k) do{ if((i)==0) asm volatile("v_mfma_f32_32x32x16_f16 %0, %1, %2, 0" : "=&v"(acc_) : "v"(A[i]), "v"(B[k])); \
                             else asm volatile("v_mfma_f32_32x32x16_f16 %0, %1, %2, %0" : "+v"(acc_) : "v"(A[i]), "v"(B[k])); }while(0)
    RA(0); RB(0); RA(1); RB(1); RA(2); RB(2); RA(3); RB(3); SB;
    WST(0); WST(1); WST(2);
    #pragma unroll
    for(int i=0;i<17;i++){
      MD(a0,i,i);
      if(i+4<17) RA(i+4);
      if(i+4<18) RB(i+4);
      SB;
    }
    #pragma unroll
    for(int i=0;i<17;i++){
      MD(a1,i,i+1);
      if(i==1) asm volatile("" : "+v"(a0));
      if(i>=1&&i<=4){ const int g=i-1; f32x4 v={a0[4*g],a0[4*g+1],a0[4*g+2],a0[4*g+3]}; *LDSP(f32x4,cvy+32*g)=v; }
      if(i>=5&&i<=10) WST(i-2);
      if(i>=11&&i<=14) UTX((i-11)>>1,(i-11)&1);
      SB;
    }
    asm volatile("s_nop 7\n\ts_nop 3" : "+v"(a1) :: "memory");
    #pragma unroll
    for(int g=0;g<4;g++){ f32x4 v={a1[4*g],a1[4*g+1],a1[4*g+2],a1[4*g+3]}; *LDSP(f32x4,cvy+128+32*g)=v; }
  } else {
    PRIO(2);
    u32x4 U[6], Us[6], T2[3], T3[4];
    #pragma unroll
    for(int q=0;q<6;q++) U[q]=*LDSP(u32x4,cvb+576*q);
    const u32 sp=*LDSP(u16,S_SP+cch*4+2*sub);
    #pragma unroll
    for(int q=0;q<6;q++) Us[q]=*LDSP(u32x4,cvc+576*q);
    SB;
    WST(0); WST(1); WST(2);
    #pragma unroll
    for(int dl=0;dl<3;dl++) T2[dl]=u32x4{*LDSP(u32,cva+96-32*dl),*LDSP(u32,cva+96-32*dl+4),*LDSP(u32,cva+96+2*CTB-32*dl),*LDSP(u32,cva+96+2*CTB-32*dl+4)};
    #pragma unroll
    for(int dl=0;dl<4;dl++){
      T3[dl][0]=*LDSP(u32,cva+96+4*CTB-32*dl); T3[dl][1]=*LDSP(u32,cva+96+4*CTB-32*dl+4);
      T3[dl][2]=*LDSP(u32,cva+128+4*CTB-32*dl); T3[dl][3]=*LDSP(u32,cva+128+4*CTB-32*dl+4);
    }
    const f32x4 z4={0.f,0.f,0.f,0.f};
    u32x4 X2[6]; u32x4 X3[2]; f32x4 Y[6], xa[6], xb[6], xc[4];
    #pragma unroll
    for(int q=0;q<6;q++){ xa[q]=mfma16h(U[q],band[0],z4); xb[q]=mfma16h(U[q],band[1],z4); if(q<4) xc[q]=mfma16h(U[q],band[2],z4); }
    band[3]*=u32x4{sp,sp,sp,sp};
    #pragma unroll
    for(int q=0;q<6;q++) Y[q]=mfma16h(Us[q],band[3],z4);
    #pragma unroll
    for(int q=0;q<6;q++){ X2[q]=u32x4{pkh(xa[q][0],xa[q][1]),pkh(xa[q][2],xa[q][3]),pkh(xb[q][0],xb[q][1]),pkh(xb[q][2],xb[q][3])};
      if(q<4){ X3[q>>1][2*(q&1)]=pkh(xc[q][0],xc[q][1]); X3[q>>1][2*(q&1)+1]=pkh(xc[q][2],xc[q][3]); } }
    #pragma unroll
    for(int dl=0;dl<3;dl++)
      #pragma unroll
      for(int q=dl;q<6;q++) Y[q]=mfma16h(T2[dl],X2[q-dl],Y[q]);
    #pragma unroll
    for(int q=2;q<6;q++) Y[q]=mfma16h(T3[q-2],X3[0],Y[q]);
    #pragma unroll
    for(int q=4;q<6;q++) Y[q]=mfma16h(T3[q-4],X3[1],Y[q]);
    #pragma unroll
    for(int q=0;q<6;q++){
      #pragma unroll
      for(int i=0;i<4;i++) *LDSP(float,cvy+1024*q+64*i)=Y[q][i];
    }
    PRIO(0);
    WST(3); WST(4); WST(5); WST(6); WST(7); WST(8);
    UTX(0,0); UTX(0,1); UTX(1,0); UTX(1,1);
  }
  LSYNC();
  const u32 eys=w>=2?6144:0;
  { auto rsHo=__builtin_amdgcn_make_buffer_rsrc((void*)(a.ws+OFF_H),0,L*D*2,0x00020000);
    f32x4 y[2];
    f32x4 yp[2][4];
    #pragma unroll
    for(int c=0;c<2;c++){ yp[c][0]=*LDSP(f32x4,eya+c*2*YDB); yp[c][1]=*LDSP(f32x4,eya+c*2*YDB+YDB); yp[c][2]=*LDSP(f32x4,eyc+c*eys*2); yp[c][3]=*LDSP(f32x4,eyc+(c*2+1)*eys); }
    SB;
    #pragma unroll
    for(int c=0;c<2;c++) y[c]=(yp[c][0]+yp[c][1])+(yp[c][2]+yp[c][3]);
    u32x2 hv[2]; f32x2 e2[4]; u32 e1[4];
    #define E4(stmt) _Pragma("unroll") for(int k=0;k<4;k++){ stmt; } SB
    E4(e2[k]=(f32x2{y[k>>1][2*(k&1)],y[k>>1][2*(k&1)+1]}*invS[k>>1]));
    E4(e1[k]=pkbf2(e2[k]));
    E4(e2[k]=(f32x2{lo16(e1[k]),hi16(e1[k])}));
    E4(e2[k]+=ut[k>>1][k&1]);
    E4(e1[k]=pkbf2(e2[k]));
    E4(e2[k]=(f32x2{lo16(e1[k]),hi16(e1[k])}));
    E4(e2[k]+=xt[k>>1][k&1]);
    E4(hv[k>>1][k&1]=pkbf2(e2[k]));
    const bool odd=tid&1;
    u32 r0=__builtin_amdgcn_update_dpp(0,(int)(odd?hv[0][0]:hv[1][0]),0xb1,0xf,0xf,false), r1=__builtin_amdgcn_update_dpp(0,(int)(odd?hv[0][1]:hv[1][1]),0xb1,0xf,0xf,false);
    u32x4 o={odd?r0:hv[0][0],odd?r1:hv[0][1],odd?hv[1][0]:r0,odd?hv[1][1]:r1};
    __builtin_amdgcn_raw_buffer_store_b128(o,rsHo,hso,0,16);
  }
  waitv<0>(); BAR();
  if(tid==0) AADD(fl+FL_H+(b&31)*128);
  u32x4 wf[29], wd[28], af[28], bq[8];
  u32 wb0=((lane>>4)&1)*4096+((lane>>5)*8+((lane>>2)&3))*256+(((2*w+((lane>>1)&1))^(2*(((lane>>2)&3)|((lane>>2)&4))))<<4)+(lane&1)*8+2*HT, wb1=wb0+57344;
  asm("" : "+v"(wb0)); asm("" : "+v"(wb1));
  #define WRA(s) (WSLOT(s)-2*HT<57344?wb0+(WSLOT(s)-2*HT):wb1+(WSLOT(s)-2*HT-57344))
  #define RDW(s) do{ u32x2 r0_=trread(WRA(s)), r1_=trread(WRA(s)+1024); wf[s]=u32x4{r0_[0],r0_[1],r1_[0],r1_[1]}; }while(0)
  RDW(0); RDW(1); RDW(2); RDW(3); RDW(4); RDW(5); RDW(6); RDW(7); RDW(8);
  LSYNC();
  const u32 voff=lane*16;
  const u32x4 rsH=mkrs(a.ws+OFF_H+(size_t)tb*4*HT,4*HT);
  const u32x4 rsD=mkrs((const char*)a.wd+w*224*(D*2)+jj*128,224*(D*2)-jj*128);
  const u32x4 rsA=mkrs(a.ws+OFF_ACT+(size_t)tb*4*112*1024,4*112*1024);
  const u32x4 rsS=mkrs(a.ws+OFF_G2+(size_t)tb*512,512);
  const u32x4 rsF=mkrs(fl,FL_SZ);
  u32 stg[4];
  f32x16 acc[4]; f32x4 csa;
  const u32 hsrc=(w>>2)*2*HT+(w&3)*1024, hdst=(w>>2)*HT+(w&3)*1024;
  #define FLAG_LOOP(PRE,SLP,val,vo,want,NN) do{ u32 c_; \
    asm volatile(PRE "s_mov_b32 %1, 0x8000\n" \
      "1:\n\tv_cmp_ne_u32 vcc, %0, %3\n\ts_cbranch_vccz 2f\n\t" \
      "s_sub_u32 %1, %1, 1\n\ts_cbranch_scc1 2f\n\ts_sleep " SLP "\n\t" \
      "buffer_load_dword %0, %2, %4, 0 offen sc1\n\ts_waitcnt vmcnt(0)\n\ts_branch 1b\n2:" \
      : "+v"(val),"=&s"(c_) : "v"(vo),"v"(want),"s"(rsF),"i"(NN) : "vcc","scc","memory"); }while(0)
  #define LD1ST "buffer_load_dword %0, %2, %4, 0 offen sc1\n\ts_waitcnt vmcnt(0)\n\t"
  #define HPOLL do{ if(w==7){ u32 fo=lane<32?FL_H+lane*128:0x7ffffff0u, fw=lane<32?7u:0u, fv=0; FLAG_LOOP(LD1ST,"2",fv,fo,fw,0); } }while(0)
  #define CPOLL(W,S) do{ if(w==(W)){ u32 fo=lane==0?FL_C+(tb*2+(S))*128:0x7ffffff0u, fw=lane==0?14u:0u, fv=0; FLAG_LOOP(LD1ST,"1",fv,fo,fw,0); } }while(0)
  u32 fav; const u32 fao=lane==0?FL_C+(tb*2)*128:0x7ffffff0u, faw=lane==0?14u:0u;
  #define LDFA asm volatile("buffer_load_dword %0, %1, %2, 0 offen sc1" : "=v"(fav) : "v"(fao), "s"(rsF))
  #define AWAIT(NN) FLAG_LOOP("s_waitcnt vmcnt(%5)\n\t","4",fav,fao,faw,NN)
  #define SIGNAL(S) do{ if(tid==0) AADD(fl+FL_C+(tb*2+(S))*128); }while(0)
  u32 sgH[7], sgD[7];
  #pragma unroll
  for(int c=0;c<7;c++){ sgH[c]=sg[c]*4096; sgD[c]=sg[c]*(32*D*2); asm volatile("" : "+s"(sgH[c]), "+s"(sgD[c])); }
  const u32 hvo=voff+hsrc, hvo2=hvo+HT;
  #define GLH(i) blds16_sc1<0>(hvo,rsH,sgH[i],hdst+(i)*4096)
  const u32 wwb=w*1024;
  #define M0W(s) m0_add<WSLOT(s)>(wwb)
  #define WSTQ(s) bldsq<0>(wvo,rsW,(sg[(s)>>2]*4+((s)&3))*(16*F*2))
  #define M0H(i) m0_add<2*HT+((i)-7)*4096>(hdst)
  #define GLHQ(i) bldsq_sc1(hvo2,rsH,sgH[(i)-7])
  u32 dvo[4];
  #pragma unroll
  for(int p=0;p<4;p++) dvo[p]=(lane>>3)*(D*2)+(((lane&7)^((lane>>2)&4))<<4)+p*8*(D*2)-(p&1)*1024;
  const u32 dwb=w*6144;
  #define DSLOT(k) ((k)>=12?57344+((k)-12)*2048:((k)%6<3?((k)%6)*2048:4*HT+((k)%6-3)*2048))
  #define M0D(k) m0_add<DSLOT(k)>(dwb)
  #define DSTQ(k,hf) bldsq<(hf)*1024>(dvo[((k)&1)*2+(hf)],rsD,sgD[(k)>>1])
  u32 db[2][2];
  db[0][0]=w*6144+((lane>>5)*4+((lane>>2)&3))*128+(((((lane>>4)&1)*2+((lane>>1)&1))^((lane>>1)&4))<<4)+(lane&1)*8; db[0][1]=db[0][0]^64;
  db[1][0]=db[0][0]+4*HT; db[1][1]=db[0][1]+4*HT;
  asm("" : "+v"(db[1][0])); asm("" : "+v"(db[1][1]));
  #define DRA(k,dt) (DSLOT(k)<4*HT?db[0][dt]+DSLOT(k):db[1][dt]+(DSLOT(k)-4*HT))
  #define RDD(dst,k,dt) do{ u32x2 r0_=trread(DRA(k,dt)), r1_=trread(DRA(k,dt)+1024); dst=u32x4{r0_[0],r0_[1],r1_[0],r1_[1]}; }while(0)
  const u32 on_=(lane&31)==0||(lane&31)==20?0x3f803f80u:0u;
  u32x4 ones={on_,on_,on_,on_};
  asm volatile("" : "+v"(ones));
  #define CS0(s) asm volatile("s_nop 1\n\tv_mfma_f32_16x16x32_bf16 %0, %1, %2, 0" : "=&v"(csa) : "v"(ones), "v"(wf[s]))
  #define CS(s) asm volatile("v_mfma_f32_16x16x32_bf16 %0, %1, %2, %0" : "+v"(csa) : "v"(ones), "v"(wf[s]))
  #define MKAX do{ asm volatile("" : "+v"(csa)); float c_=csa[0], hi_=rbf(c_), r1_=c_-hi_, mi_=rbf(r1_); \
    wf[28]=u32x4{lane<32?pkbf(hi_,mi_):0u,lane<32?pkbf(r1_-mi_,0.f):0u,0u,0u}; asm volatile("" : "+v"(wf[28])); }while(0)
  const u32 svo=(lane&31)*4;
  #define LDST(k) asm volatile("buffer_load_dword %0, %1, %2, 0 offen offset:%3 sc1" : "=v"(stg[k]) : "v"(svo), "s"(rsS), "i"((((k)&1)*2+((k)>>1))*128))
  u32 ba0=(lane>>5)*512+((lane>>2)&3)*64+((lane>>4)&1)*32+(lane&3)*8, bt2=ba0+2*HT;
  asm("" : "+v"(bt2));
  #define RDT(T,s,r) do{ u32x2 r0_=trread(((T)<2?ba0:bt2)+((T)&1)*HT+(s)*1024), r1_=trread(((T)<2?ba0:bt2)+((T)&1)*HT+(s)*1024+256); bq[r]=u32x4{r0_[0],r0_[1],r1_[0],r1_[1]}; }while(0)
  #define MM0(T,s,r) asm volatile("v_mfma_f32_32x32x16_bf16 %0, %1, %2, 0" : "=&v"(acc[T]) : "v"(wf[s]), "v"(bq[r]))
  #define MM(T,s,r) asm volatile("v_mfma_f32_32x32x16_bf16 %0, %1, %2, %0" : "+v"(acc[T]) : "v"(wf[s]), "v"(bq[r]))
  #define MMC(T) do{ u32 nm_=(stg[T]&0xffffu)^0x8000u; nm_|=nm_<<16; u32x4 bx_={nm_,nm_,nm_,nm_}; \
    asm volatile("s_nop 1\n\tv_mfma_f32_32x32x16_bf16 %0, %1, %2, %0" : "+v"(acc[T]) : "v"(wf[28]), "v"(bx_)); }while(0)
  #define STATS_WAIT(NN) do{ u32 t_; u32 c_; \
    asm volatile("s_waitcnt vmcnt(%8)\n\ts_mov_b32 %5, 0x8000\n" \
      "1:\n\tv_min3_u32 %4, %0, %1, %2\n\tv_min_u32 %4, %4, %3\n\tv_cmp_eq_u32 vcc, 0, %4\n\ts_cbranch_vccz 2f\n\t" \
      "s_sub_u32 %5, %5, 1\n\ts_cbranch_scc1 2f\n\ts_sleep 2\n\t" \
      "buffer_load_dword %0, %6, %7, 0 offen sc1\n\tbuffer_load_dword %1, %6, %7, 0 offen offset:256 sc1\n\t" \
      "buffer_load_dword %2, %6, %7, 0 offen offset:128 sc1\n\tbuffer_load_dword %3, %6, %7, 0 offen offset:384 sc1\n\t" \
      "s_waitcnt vmcnt(0)\n\ts_branch 1b\n2:" \
      : "+v"(stg[0]),"+v"(stg[1]),"+v"(stg[2]),"+v"(stg[3]),"=&v"(t_),"=&s"(c_) : "v"(svo),"s"(rsS),"i"(NN) : "vcc","scc"); }while(0)
  const float c0=-2.f*0.7978845608028654f*1.4426950408889634f, c1=c0*0.044715f;
  float ek0,ek1,es2,ez0,ez1,eg0,eg1; u32x4 eo;
  #define OPQ(x) asm volatile("" : "+v"(x))
  #define OPQ2(x,y) asm volatile("" : "+v"(x), "+v"(y))
  #define EPI(T,k) do{ constexpr int g_=((k)/3)&3, st_=(k)%3; \
    if((k)==0){ OPQ(acc[T]); float sd_=hi16(stg[T]); float ri_=__builtin_amdgcn_rcpf(sd_); es2=sd_*sd_; ek0=c0*ri_; OPQ(ek0); ek1=(c1*ri_)*(ri_*ri_); } \
    if(st_==0){ if((k)>0){ eo[(k)/3-1]=pkbf(eg0,eg1); OPQ(eo[(k)/3-1]); } \
      if((k)<12){ float g0_=acc[T][2*g_], g1_=acc[T][2*g_+1]; ez0=g0_*__builtin_fmaf(g0_*g0_,ek1,ek0); ez1=g1_*__builtin_fmaf(g1_*g1_,ek1,ek0); OPQ2(ez0,ez1); } } \
    else if(st_==1){ float e0_=__builtin_amdgcn_exp2f(ez0), e1_=__builtin_amdgcn_exp2f(ez1); eg0=acc[T][2*g_]*acc[T][8+2*g_]; eg1=acc[T][2*g_+1]*acc[T][9+2*g_]; \
      ez0=__builtin_fmaf(e0_,es2,es2); ez1=__builtin_fmaf(e1_,es2,es2); asm volatile("" : "+v"(ez0), "+v"(ez1), "+v"(eg0), "+v"(eg1)); } \
    else { float r0_=__builtin_amdgcn_rcpf(ez0), r1_=__builtin_amdgcn_rcpf(ez1); eg0*=r0_; eg1*=r1_; OPQ2(eg0,eg1); } }while(0)
  #define EPIL(T) do{ asm volatile("s_nop 7\n\ts_nop 3" : "+v"(acc[T])); float sd_=hi16(stg[T]); float ri_=__builtin_amdgcn_rcpf(sd_); float k0_=c0*ri_, k1_=(c1*ri_)*(ri_*ri_), s2_=sd_*sd_; \
    const f32x2 K0={k0_,k0_}, K1={k1_,k1_}, S2={s2_,s2_}; \
    _Pragma("unroll") for(int g_=0;g_<4;g_++){ f32x2 g2_={acc[T][2*g_],acc[T][2*g_+1]}, u2_={acc[T][8+2*g_],acc[T][9+2*g_]}; \
      f32x2 z_=g2_*__builtin_elementwise_fma(g2_*g2_,K1,K0); f32x2 e_={__builtin_amdgcn_exp2f(z_[0]),__builtin_amdgcn_exp2f(z_[1])}; \
      f32x2 d_=__builtin_elementwise_fma(e_,S2,S2); f32x2 r_={__builtin_amdgcn_rcpf(d_[0]),__builtin_amdgcn_rcpf(d_[1])}; eo[g_]=pkbf2((g2_*u2_)*r_); } }while(0)
  #define STACT(T) asm volatile("buffer_store_dwordx4 %0, %1, %2, %3 offen sc1\n\ts_nop 1" :: "v"(eo), "v"(voff), "s"(rsA), "s"((u32)(((((T)&1)*2+((T)>>1))*112+j*8+w)*1024)) : "memory")
  u32x4 hres; u32 hra;
  { const int gs_=jj-gr0; hra=ba0+((w&1)*2+half)*HT+((gs_<0?gs_+7:gs_)*4+(w>>1))*1024; PIN(hra); }
  #define HRES do{ u32x2 r0_=trread(hra), r1_=trread(hra+256); hres=u32x4{r0_[0],r0_[1],r1_[0],r1_[1]}; }while(0)
  f32x16 o[2][2];
  #define OINIT do{ const int e_=(lane&31)-16*((w>>1)&1)-8*(lane>>5); u32x4 sel; \
    _Pragma("unroll") for(int i=0;i<4;i++) sel[i]= e_==2*i?0x3f80u:(e_==2*i+1?0x3f800000u:0u); \
    _Pragma("unroll") for(int tt=0;tt<2;tt++) _Pragma("unroll") for(int dt=0;dt<2;dt++){ const u32 mk=((w&1)==tt&&(w>>2)==dt)?~0u:0u; u32x4 s_=sel&u32x4{mk,mk,mk,mk}; \
      asm volatile("s_nop 1\n\tv_mfma_f32_32x32x16_bf16 %0, %1, %2, 0" : "=&v"(o[tt][dt]) : "v"(hres), "v"(s_)); } }while(0)
  #define BPOLL CPOLL(0,1)
  const u32 aso=(half*2*112+w*14)*1024;
  #define LDA(tt,k) bld16<((k)&1)*1024>(af[(tt)*14+(k)],voff,rsA,aso+sg[(k)>>1]*2048+(tt)*114688u)
  #define MMD(tt,dt,A_,B_) asm volatile("v_mfma_f32_32x32x16_bf16 %0, %1, %2, %0" : "+v"(o[tt][dt]) : "v"(A_), "v"(B_))
  #define RW(tt,dt,I) put_tidf<(I)*2048+(dt)*32768+(tt)*1796>(o[tt][dt][I]);
  #define RW8(tt,dt,B) RW(tt,dt,B) RW(tt,dt,B+1) RW(tt,dt,B+2) RW(tt,dt,B+3) RW(tt,dt,B+4) RW(tt,dt,B+5) RW(tt,dt,B+6) RW(tt,dt,B+7)
  #define REDM(tt) set_m0(w*256+(tt)*63740)
  #define REDW(tt) do{ asm volatile("s_nop 7\n\ts_nop 7"); set_m0(w*256+(tt)*63740); RW8(tt,0,0) RW8(tt,0,8) RW8(tt,1,0) RW8(tt,1,8) }while(0)
  const u32x4 rsO=mkrs(a.out+(size_t)(tb*128+half*64)*D+jj*64,64*D*2-jj*128);
  #define REDR(tt) do{ const int d4=tid&7, dt=(tid>>3)&1, hh=(tid>>4)&1, rg=tid>>5; \
    const u32 rr=(tt)*65536+rg*2048+dt*32768+hh*128+d4*16; \
    f32x4 s0=*LDSP(f32x4,rr); \
    _Pragma("unroll") for(int q=1;q<8;q++) s0+=*LDSP(f32x4,rr+q*256); \
    u32x2 ov={pkbf(s0[0],s0[1]),pkbf(s0[2],s0[3])}; \
    const u32 oo=((tt)*32+(rg&3)+8*(rg>>2)+4*hh)*(D*2)+dt*64+d4*8; \
    asm volatile("buffer_store_dwordx2 %0, %1, %2, 0 offen" :: "v"(ov), "v"(oo), "s"(rsO) : "memory"); }while(0)
  WST(9); WST(10); WST(11); WST(12); WST(13); WST(14); WST(15); WST(16); WST(17); WST(18); WST(19); WST(20); WST(21); CS0(0); CS(1); CS(2); CS(3); CS(4);
  CS(5); CS(6); CS(7); CS(8); HPOLL; waitv<0>(); BAR();
  GLH(0); GLH(1); GLH(2); GLH(3); GLH(4); GLH(5); GLH(6); RDW(9); RDW(10); RDW(11); RDW(12); RDW(13); RDW(14); RDW(15); RDW(16); RDW(17); RDW(18); RDW(19);
  RDW(20); RDW(21); SB;
  CS(9); CS(10); CS(11); CS(12); CS(13); CS(14); CS(15); CS(16); CS(17); CS(18); CS(19); CS(20); CS(21); waitv<0>(); LSYNC();
  RDT(0,1,1); RDT(0,0,0); RDT(0,3,3); RDT(0,2,2); WST(22); WST(23); M0W(24); PRIO(3); MM0(0,0,0); WSTQ(24); SB;
  M0W(25); MM(0,1,1); RDT(0,5,5); RDT(0,4,4); WSTQ(25); SB;
  M0W(26); MM(0,2,2); WSTQ(26); SB;
  M0W(27); MM(0,3,3); RDT(0,7,7); RDT(0,6,6); WSTQ(27); SB;
  M0H(7); MM(0,4,4); GLHQ(7); SB;
  PRIO(2); MM(0,5,5); RDT(0,9,1); RDT(0,8,0); SB;
  M0H(8); MM(0,6,6); GLHQ(8); SB;
  MM(0,7,7); RDT(0,11,3); RDT(0,10,2); SB;
  M0H(9); MM(0,8,0); GLHQ(9); SB;
  MM(0,9,1); RDT(0,13,5); RDT(0,12,4); SB;
  M0H(10); PRIO(1); MM(0,10,2); GLHQ(10); SB;
  MM(0,11,3); RDT(0,15,7); RDT(0,14,6); SB;
  M0H(11); MM(0,12,4); GLHQ(11); SB;
  MM(0,13,5); RDT(0,17,1); RDT(0,16,0); SB;
  M0H(12); MM(0,14,6); GLHQ(12); SB;
  PRIO(0); MM(0,15,7); RDT(0,19,3); RDT(0,18,2); SB;
  M0H(13); MM(0,16,0); GLHQ(13); SB;
  MM(0,17,1); RDT(0,21,5); RDT(0,20,4); SB;
  MM(0,18,2); SB;
  MM(0,19,3); RDT(0,23,7); RDT(0,22,6); waitv<7>(); BAR();
  RDW(22); RDW(23); SB;
  PRIO(3); MM(0,20,4); RDW(24); RDW(25); SB;
  MM(0,21,5); RDT(0,25,1); RDT(0,24,0); RDW(26); RDW(27); SB;
  MM(0,22,6); CS(22); SB;
  PRIO(2); MM(0,23,7); RDT(0,27,3); RDT(0,26,2); CS(23); SB;
  MM(0,24,0); CS(24); SB;
  MM(0,25,1); RDT(1,1,5); RDT(1,0,4); CS(25); SB;
  MM(0,26,2); CS(26); SB;
  PRIO(1); MM(0,27,3); RDT(1,3,7); RDT(1,2,6); CS(27); SB;
  MM0(1,0,4); SB;
  MM(1,1,5); RDT(1,5,1); RDT(1,4,0); LDST(0); LDST(1); LDST(2); LDST(3); SB;
  PRIO(0); MM(1,2,6); MKAX; SB;
  MM(1,3,7); RDT(1,7,3); RDT(1,6,2); SB;
  MM(1,4,0); SB;
  MM(1,5,1); RDT(1,9,5); RDT(1,8,4); waitv<4>(); BAR();
  SB;
  PRIO(3); MM(1,6,2); HRES; SB;
  M0D(3); MM(1,7,3); RDT(1,11,7); RDT(1,10,6); DSTQ(3,0); SB;
  MM(1,8,4); STATS_WAIT(1); MMC(0); DSTQ(3,1); SB;
  MM(1,9,5); RDT(1,13,1); RDT(1,12,0); SB;
  MM(1,10,6); EPI(0,0); SB;
  MM(1,11,7); RDT(1,15,3); RDT(1,14,2); EPI(0,1); SB;
  PRIO(2); MM(1,12,0); SB;
  M0D(4); MM(1,13,1); RDT(1,17,5); RDT(1,16,4); DSTQ(4,0); EPI(0,2); SB;
  MM(1,14,2); DSTQ(4,1); EPI(0,3); SB;
  MM(1,15,3); RDT(1,19,7); RDT(1,18,6); SB;
  MM(1,16,4); EPI(0,4); SB;
  MM(1,17,5); RDT(1,21,1); RDT(1,20,0); EPI(0,5); SB;
  MM(1,18,6); SB;
  M0D(5); PRIO(1); MM(1,19,7); RDT(1,23,3); RDT(1,22,2); DSTQ(5,0); EPI(0,6); SB;
  MM(1,20,0); DSTQ(5,1); SB;
  MM(1,21,1); RDT(1,25,5); RDT(1,24,4); EPI(0,7); SB;
  MM(1,22,2); EPI(0,8); SB;
  MM(1,23,3); RDT(1,27,7); RDT(1,26,6); SB;
  MM(1,24,4); EPI(0,9); SB;
  PRIO(0); MM(1,25,5); RDT(2,1,1); RDT(2,0,0); EPI(0,10); SB;
  MM(1,26,6); SB;
  MM(1,27,7); RDT(2,3,3); RDT(2,2,2); MMC(1); EPI(0,11); SB;
  MM0(2,0,0); SB;
  MM(2,1,1); RDT(2,5,5); RDT(2,4,4); EPI(0,12); STACT(0); SB;
  MM(2,2,2); EPI(1,0); SB;
  MM(2,3,3); RDT(2,7,7); RDT(2,6,6); BAR();
  SB;
  M0D(0); PRIO(3); MM(2,4,4); DSTQ(0,0); EPI(1,1); SB;
  MM(2,5,5); RDT(2,9,1); RDT(2,8,0); DSTQ(0,1); SB;
  MM(2,6,6); EPI(1,2); SB;
  MM(2,7,7); RDT(2,11,3); RDT(2,10,2); SB;
  M0D(1); MM(2,8,0); DSTQ(1,0); EPI(1,3); SB;
  MM(2,9,1); RDT(2,13,5); RDT(2,12,4); DSTQ(1,1); SB;
  MM(2,10,2); waitv<9>(); RDD(wd[6],3,0); EPI(1,4); SB;
  MM(2,11,3); RDT(2,15,7); RDT(2,14,6); RDD(wd[7],3,1); SB;
  M0D(2); MM(2,12,4); DSTQ(2,0); EPI(1,5); SB;
  PRIO(2); MM(2,13,5); RDT(2,17,1); RDT(2,16,0); DSTQ(2,1); waitv<9>(); RDD(wd[8],4,0); SB;
  MM(2,14,6); RDD(wd[9],4,1); EPI(1,6); SB;
  MM(2,15,7); RDT(2,19,3); RDT(2,18,2); SB;
  MM(2,16,0); waitv<7>(); RDD(wd[10],5,0); EPI(1,7); SB;
  MM(2,17,1); RDT(2,21,5); RDT(2,20,4); RDD(wd[11],5,1); SB;
  M0D(9); MM(2,18,2); DSTQ(9,0); EPI(1,8); SB;
  MM(2,19,3); RDT(2,23,7); RDT(2,22,6); DSTQ(9,1); SB;
  MM(2,20,4); EPI(1,9); SB;
  M0D(10); MM(2,21,5); RDT(2,25,1); RDT(2,24,0); DSTQ(10,0); SB;
  PRIO(1); MM(2,22,6); DSTQ(10,1); EPI(1,10); SB;
  MM(2,23,7); RDT(2,27,3); RDT(2,26,2); SB;
  M0D(11); MM(2,24,0); DSTQ(11,0); EPI(1,11); SB;
  MM(2,25,1); RDT(3,1,5); RDT(3,0,4); DSTQ(11,1); SB;
  MM(2,26,2); EPI(1,12); STACT(1); SB;
  MM(2,27,3); RDT(3,3,7); RDT(3,2,6); MMC(2); SB;
  MM0(3,0,4); waitv<11>(); RDD(wd[0],0,0); SB;
  MM(3,1,5); RDT(3,5,1); RDT(3,4,0); RDD(wd[1],0,1); SB;
  MM(3,2,6); EPI(2,0); SB;
  PRIO(0); MM(3,3,7); RDT(3,7,3); RDT(3,6,2); waitv<9>(); RDD(wd[2],1,0); SB;
  MM(3,4,0); RDD(wd[3],1,1); EPI(2,1); SB;
  MM(3,5,1); RDT(3,9,5); RDT(3,8,4); SB;
  MM(3,6,2); waitv<7>(); RDD(wd[4],2,0); EPI(2,2); SB;
  MM(3,7,3); RDT(3,11,7); RDT(3,10,6); RDD(wd[5],2,1); SB;
  M0D(6); MM(3,8,4); DSTQ(6,0); EPI(2,3); SB;
  MM(3,9,5); RDT(3,13,1); RDT(3,12,0); DSTQ(6,1); SB;
  MM(3,10,6); EPI(2,4); SB;
  M0D(7); MM(3,11,7); RDT(3,15,3); RDT(3,14,2); DSTQ(7,0); waitv<3>(); BAR(); SIGNAL(0);
  SB;
  PRIO(3); MM(3,12,0); DSTQ(7,1); EPI(2,5); SB;
  MM(3,13,1); RDT(3,17,5); RDT(3,16,4); SB;
  M0D(8); MM(3,14,2); DSTQ(8,0); EPI(2,6); SB;
  MM(3,15,3); RDT(3,19,7); RDT(3,18,6); DSTQ(8,1); SB;
  PRIO(2); MM(3,16,4); EPI(2,7); SB;
  MM(3,17,5); RDT(3,21,1); RDT(3,20,0); SB;
  MM(3,18,6); EPI(2,8); SB;
  MM(3,19,7); RDT(3,23,3); RDT(3,22,2); SB;
  PRIO(1); MM(3,20,0); EPI(2,9); SB;
  MM(3,21,1); RDT(3,25,5); RDT(3,24,4); SB;
  MM(3,22,2); EPI(2,10); SB;
  MM(3,23,3); RDT(3,27,7); RDT(3,26,6); SB;
  PRIO(0); MM(3,24,4); EPI(2,11); SB;
  MM(3,25,5); LDFA; SB;
  MM(3,26,6); EPI(2,12); STACT(2); SB;
  MM(3,27,7); MMC(3); SB;
  EPIL(3); STACT(3); AWAIT(2); LDA(0,3); LDA(0,4); LDA(0,5); LDA(0,0); LDA(0,1); LDA(0,2); OINIT; waitv<20>(); RDD(wd[18],9,0); RDD(wd[19],9,1); waitv<18>();
  RDD(wd[20],10,0); RDD(wd[21],10,1); waitv<16>(); RDD(wd[22],11,0); RDD(wd[23],11,1); waitv<6>(); LSYNC();
  SIGNAL(1); M0D(12); asm volatile("s_nop 0"); DSTQ(12,0); DSTQ(12,1); LDA(0,9); LDA(0,10); waitw<9>(af[3]); MMD(0,0,af[3],wd[6]); M0D(13); asm volatile("s_nop 0");
  DSTQ(13,0); MMD(0,1,af[3],wd[7]); DSTQ(13,1); SB;
  waitw<10>(af[4]); MMD(0,0,af[4],wd[8]); LDA(0,11); MMD(0,1,af[4],wd[9]); LDA(0,6); SB;
  waitw<11>(af[5]); MMD(0,0,af[5],wd[10]); LDA(0,7); MMD(0,1,af[5],wd[11]); LDA(0,8); SB;
  waitw<12>(af[0]); MMD(0,0,af[0],wd[0]); LDA(0,12); MMD(0,1,af[0],wd[1]); LDA(0,13); SB;
  waitw<13>(af[1]); MMD(0,0,af[1],wd[2]); MMD(0,1,af[1],wd[3]); SB;
  waitw<12>(af[2]); MMD(0,0,af[2],wd[4]); MMD(0,1,af[2],wd[5]); SB;
  waitw<9>(af[9]); MMD(0,0,af[9],wd[18]); MMD(0,1,af[9],wd[19]); SB;
  waitv<25>(); RDD(wd[12],6,0); RDD(wd[13],6,1); waitw<8>(af[10]); MMD(0,0,af[10],wd[20]); MMD(0,1,af[10],wd[21]); SB;
  waitv<23>(); RDD(wd[14],7,0); RDD(wd[15],7,1); waitw<5>(af[11]); MMD(0,0,af[11],wd[22]); BPOLL; BAR();
  LDA(1,3); LDA(1,4); LDA(1,5); LDA(1,0); MMD(0,1,af[11],wd[23]); SB;
  waitv<25>(); RDD(wd[16],8,0); RDD(wd[17],8,1); waitw<8>(af[6]); MMD(0,0,af[6],wd[12]); LDA(1,1); MMD(0,1,af[6],wd[13]); SB;
  waitv<15>(); RDD(wd[24],12,0); RDD(wd[25],12,1); waitw<8>(af[7]); MMD(0,0,af[7],wd[14]); LDA(1,2); MMD(0,1,af[7],wd[15]); SB;
  waitv<12>(); RDD(wd[26],13,0); RDD(wd[27],13,1); waitw<8>(af[8]); MMD(0,0,af[8],wd[16]); LDA(1,9); MMD(0,1,af[8],wd[17]); SB;
  waitw<8>(af[12]); MMD(0,0,af[12],wd[24]); LDA(1,10); MMD(0,1,af[12],wd[25]); SB;
  LSYNC();
  waitw<8>(af[13]); MMD(0,0,af[13],wd[26]); LDA(1,11); MMD(0,1,af[13],wd[27]); SB;
  waitw<8>(af[17]); MMD(1,0,af[17],wd[6]); LDA(1,6); MMD(1,1,af[17],wd[7]); SB;
  REDM(0); waitw<8>(af[18]); MMD(1,0,af[18],wd[8]); LDA(1,7); MMD(1,1,af[18],wd[9]); SB;
  RW8(0,0,0) SB;
  waitw<8>(af[19]); MMD(1,0,af[19],wd[10]); LDA(1,8); MMD(1,1,af[19],wd[11]); SB;
  RW8(0,0,8) SB;
  waitw<8>(af[14]); MMD(1,0,af[14],wd[0]); LDA(1,12); MMD(1,1,af[14],wd[1]); SB;
  RW8(0,1,0) SB;
  waitw<8>(af[15]); MMD(1,0,af[15],wd[2]); LDA(1,13); MMD(1,1,af[15],wd[3]); SB;
  RW8(0,1,8) SB;
  waitw<8>(af[16]); MMD(1,0,af[16],wd[4]); MMD(1,1,af[16],wd[5]); SB;
  waitw<7>(af[23]); MMD(1,0,af[23],wd[18]); MMD(1,1,af[23],wd[19]); SB;
  LSYNC();
  waitw<6>(af[24]); MMD(1,0,af[24],wd[20]); MMD(1,1,af[24],wd[21]); SB;
  REDR(0); SB;
  waitw<6>(af[25]); MMD(1,0,af[25],wd[22]); MMD(1,1,af[25],wd[23]); SB;
  waitw<5>(af[20]); MMD(1,0,af[20],wd[12]); MMD(1,1,af[20],wd[13]); SB;
  waitw<4>(af[21]); MMD(1,0,af[21],wd[14]); MMD(1,1,af[21],wd[15]); SB;
  waitw<3>(af[22]); MMD(1,0,af[22],wd[16]); MMD(1,1,af[22],wd[17]); SB;
  waitw<2>(af[26]); MMD(1,0,af[26],wd[24]); MMD(1,1,af[26],wd[25]); SB;
  waitw<1>(af[27]); MMD(1,0,af[27],wd[26]); MMD(1,1,af[27],wd[27]); SB;
  REDW(1); LSYNC();
  REDR(1);
}

extern "C" void kernel_launch(void* const* d_in, const int* in_sizes, int n_in,
                              void* d_out, int out_size, void* d_ws, size_t ws_size,
                              hipStream_t stream) {
  SArgs sa;
  sa.x=(const u16*)d_in[0]; sa.Are=(const u16*)d_in[1]; sa.Aim=(const u16*)d_in[2]; sa.Bre=(const u16*)d_in[3]; sa.Bim=(const u16*)d_in[4];
  sa.Cre=(const u16*)d_in[5]; sa.Cim=(const u16*)d_in[6]; sa.logdt=(const u16*)d_in[7]; sa.Dskip=(const u16*)d_in[8];
  sa.wg=(const u16*)d_in[10]; sa.wu=(const u16*)d_in[11]; sa.wd=(const u16*)d_in[12]; sa.ws=(char*)d_ws;
  sa.out=(u16*)d_out;
  k_all<<<256,512,0,stream>>>(sa);
}
```
